```python
import torch

def _softmax(x, axis=-1):
    return torch.softmax(x, dim=axis)
def _gelu(x):
    return torch.nn.functional.gelu(x, approximate="tanh")
def _silu(x):
    return torch.nn.functional.silu(x)
def _sigmoid(x):
    return torch.sigmoid(x)
def _layernorm(x, axis=-1, eps=1e-5):
    m = x.mean(dim=axis, keepdim=True)
    v = x.var(dim=axis, keepdim=True, unbiased=False)
    return (x - m) / torch.sqrt(v + eps)
def _scatter_add(tgt, idx, src, axis=0):
    return tgt.clone().index_add_(axis, idx, src)
def _segment_sum(src, seg, n):
    out = torch.zeros((n,) + src.shape[1:], dtype=src.dtype)
    return out.index_add_(0, seg, src)
def _pad(x, pw):
    flat = tuple(v for pair in reversed(pw) for v in pair)
    return torch.nn.functional.pad(x, flat)

kernel_name = 'sequential_preLN__ssm_conv_s4d__glu_gated_swiglu__prompt'

def setup_inputs():

    _w = (lambda a: (lambda t: t.to(torch.bfloat16) if t.is_floating_point() else t)(torch.as_tensor(a)))
    def _A_setup():
        Bsz, L, D, N = 1, 2048, 512, 128
        return {
            "u": _w((lambda *s: torch.randn(*s, dtype=torch.bfloat16) * 0.05)(Bsz, L, D)),
            "log_A_real": _w((lambda *s: torch.randn(*s, dtype=torch.bfloat16) * 6.4)(D, N) * 0.5),
            "A_imag": _w((lambda *s: torch.randn(*s, dtype=torch.bfloat16) * 0.05)(D, N) * 3.14),
            "C_real": _w((lambda *s: torch.randn(*s, dtype=torch.bfloat16) * 0.05)(D, N) / N ** 0.5),
            "C_imag": _w((lambda *s: torch.randn(*s, dtype=torch.bfloat16) * 0.05)(D, N) / N ** 0.5),
            "log_dt": _w((lambda *s: torch.randn(*s, dtype=torch.bfloat16) * 0.05)(D) * 0.5 - 4.0),
            "D_skip": _w((lambda *s: torch.randn(*s, dtype=torch.bfloat16) * 0.05)(D)),
        }
    _A_in = _A_setup()

    def _B_setup():
        B, S, D, F = 1, 2048, 512, 1792
        return {
            "x": _w((lambda *s: torch.randn(*s, dtype=torch.bfloat16) * 0.05)(B, S, D)),
            "w1": _w((lambda *s: torch.randn(*s, dtype=torch.bfloat16) * 0.4)(D, F) * (2.0 / D) ** 0.5),
            "w3": _w((lambda *s: torch.randn(*s, dtype=torch.bfloat16) * 0.4)(D, F) * (2.0 / D) ** 0.5),
            "w2": _w((lambda *s: torch.randn(*s, dtype=torch.bfloat16) * 0.4)(F, D) * (2.0 / F) ** 0.5),
        }
    _B_in = _B_setup()

    return {
        "A_u": _A_in["u"],
        "A_log_A_real": _A_in["log_A_real"],
        "A_A_imag": _A_in["A_imag"],
        "A_C_real": _A_in["C_real"],
        "A_C_imag": _A_in["C_imag"],
        "A_log_dt": _A_in["log_dt"],
        "A_D_skip": _A_in["D_skip"],
        "B_x": _B_in["x"],
        "B_w1": _B_in["w1"],
        "B_w3": _B_in["w3"],
        "B_w2": _B_in["w2"],
    }

def reference(A_u, A_log_A_real, A_A_imag, A_C_real, A_C_imag, A_log_dt, A_D_skip, B_x, B_w1, B_w3, B_w2):
    def _A_ref(u, log_A_real, A_imag, C_real, C_imag, log_dt, D_skip):
        L = u.shape[1]
        dt = torch.exp(log_dt)
        A = -torch.exp(log_A_real) + 1j * A_imag
        C = (C_real + 1j * C_imag) * dt[:, None]
        pos = (lambda x, dt: x.to(dt))(torch.arange(L), log_dt.dtype)
        P = torch.exp((A * dt[:, None])[:, :, None] * pos[None, None, :])
        K = 2.0 * torch.einsum("dn,dnl->dl", C, P).real
        L2 = 2 * L
        u_t = (lambda x, *d: x.permute(*d))(u, 0, 2, 1)
        Kf = (lambda x, *a, **k: torch.fft.rfft(x.float(), *a, **k))(K, L2)
        Uf = (lambda x, *a, **k: torch.fft.rfft(x.float(), *a, **k))(u_t, L2)
        y = (lambda x, *a, **k: torch.fft.irfft(x, *a, **k).to(torch.bfloat16))(Uf * Kf[None, :, :], L2)[:, :, :L]
        y = (lambda x, *d: x.permute(*d))(y, 0, 2, 1)
        return y + u * D_skip[None, None, :]

    def _B_ref(x, w1, w3, w2):
        gate = torch.einsum("bsd,df->bsf", x, w1)
        up = torch.einsum("bsd,df->bsf", x, w3)
        h = _silu(gate) * up
        out = torch.einsum("bsf,fd->bsd", h, w2)
        return out

    x = A_u
    h = x + _A_ref(_layernorm(x, axis=-1), A_log_A_real, A_A_imag, A_C_real, A_C_imag, A_log_dt, A_D_skip)
    h2 = h + _B_ref(_layernorm(h, axis=-1), B_w1, B_w3, B_w2)
    return h2
```

```cpp
#include <hip/hip_runtime.h>
#include <hip/hip_bf16.h>
#include <stdint.h>
typedef unsigned short u16;
#define L_ 2048
#define D_ 512
#define N_ 128
#define F_ 1792
__device__ __forceinline__ float bf2f(u16 v){ return __uint_as_float(((uint32_t)v)<<16); }
__device__ __forceinline__ u16 f2bf(float f){ uint32_t u=__float_as_uint(f); u = u + 0x7FFF + ((u>>16)&1); return (u16)(u>>16); }
__device__ __forceinline__ float rbf(float f){ return bf2f(f2bf(f)); }

__global__ void k_ln(const u16* x, u16* u){
  int row = blockIdx.x; int t = threadIdx.x;
  float v[8]; float s=0;
  for(int i=0;i<8;i++){ v[i]=bf2f(x[row*D_+t*8+i]); s+=v[i]; }
  for(int o=32;o>0;o>>=1) s+=__shfl_xor(s,o);
  float m=s/D_; float q=0;
  for(int i=0;i<8;i++){ float d=v[i]-m; q+=d*d; }
  for(int o=32;o>0;o>>=1) q+=__shfl_xor(q,o);
  float var=rbf(q/D_); float mb=rbf(m);
  float sd=rbf(sqrtf(rbf(var+1e-5f)));
  for(int i=0;i<8;i++){ u[row*D_+t*8+i]=f2bf(rbf(v[i]-mb)/sd); }
}
__global__ void k_kgen(const u16* lar,const u16* aim,const u16* cr,const u16* ci,const u16* ldt,float* K){
  int d=blockIdx.y; int l=blockIdx.x*blockDim.x+threadIdx.x;
  float dt=rbf(expf(bf2f(ldt[d])));
  float pos=rbf((float)l);
  float acc=0;
  for(int n=0;n<N_;n++){
    float ar=-rbf(expf(bf2f(lar[d*N_+n])))*dt; float ai=bf2f(aim[d*N_+n])*dt;
    float c_r=bf2f(cr[d*N_+n])*dt, c_i=bf2f(ci[d*N_+n])*dt;
    float e=expf(ar*pos); float sn,cs; sincosf(ai*pos,&sn,&cs);
    acc+=c_r*e*cs-c_i*e*sn;
  }
  K[d*L_+l]=2.f*acc;
}
__global__ void k_conv(const u16* x,const u16* u,const float* K,const u16* Dsk,u16* h){
  int l=blockIdx.x; int d=threadIdx.x;
  float acc=0;
  for(int j=0;j<=l;j++) acc+=K[d*L_+j]*bf2f(u[(l-j)*D_+d]);
  float uu=bf2f(u[l*D_+d]);
  float a=rbf(rbf(acc)+rbf(uu*bf2f(Dsk[d])));
  h[l*D_+d]=f2bf(bf2f(x[l*D_+d])+a);
}
__global__ void k_g1(const u16* a,const u16* w1,const u16* w3,u16* hm){
  int l=blockIdx.y; int f=blockIdx.x*blockDim.x+threadIdx.x;
  float g=0,p=0;
  for(int k=0;k<D_;k++){ float av=bf2f(a[l*D_+k]); g+=av*bf2f(w1[k*F_+f]); p+=av*bf2f(w3[k*F_+f]); }
  g=rbf(g); p=rbf(p);
  float s=rbf(g/(1.f+expf(-g)));
  hm[l*F_+f]=f2bf(s*p);
}
__global__ void k_g2(const u16* hm,const u16* w2,const u16* h,u16* out){
  int l=blockIdx.x; int d=threadIdx.x;
  float acc=0;
  for(int k=0;k<F_;k++) acc+=bf2f(hm[l*F_+k])*bf2f(w2[k*D_+d]);
  out[l*D_+d]=f2bf(bf2f(h[l*D_+d])+rbf(acc));
}
extern "C" void kernel_launch(void* const* d_in, const int* in_sizes, int n_in,
                              void* d_out, int out_size, void* d_ws, size_t ws_size,
                              hipStream_t stream) {
  const u16* x=(const u16*)d_in[0];
  char* ws=(char*)d_ws;
  u16* u=(u16*)ws; float* K=(float*)(ws+(4<<20)); u16* h=(u16*)(ws+(8<<20)); u16* a=(u16*)(ws+(12<<20)); u16* hm=(u16*)(ws+(16<<20));
  k_ln<<<L_,64,0,stream>>>(x,u);
  k_kgen<<<dim3(L_/256,D_),256,0,stream>>>((const u16*)d_in[1],(const u16*)d_in[2],(const u16*)d_in[3],(const u16*)d_in[4],(const u16*)d_in[5],K);
  k_conv<<<L_,D_,0,stream>>>(x,u,K,(const u16*)d_in[6],h);
  k_ln<<<L_,64,0,stream>>>(h,a);
  k_g1<<<dim3(F_/256,L_),256,0,stream>>>(a,(const u16*)d_in[8],(const u16*)d_in[9],hm);
  k_g2<<<L_,D_,0,stream>>>(hm,(const u16*)d_in[10],h,(u16*)d_out);
}
```

```cpp
#include <hip/hip_runtime.h>
#include <stdint.h>
#define DI __device__ __forceinline__
#define GAS __attribute__((address_space(1)))
#define LAS __attribute__((address_space(3)))
typedef unsigned u32; typedef unsigned long long u64; typedef unsigned short u16;
typedef u32 v4u __attribute__((ext_vector_type(4)));
typedef u32 v2u __attribute__((ext_vector_type(2)));
typedef float f2 __attribute__((ext_vector_type(2)));
typedef float f4 __attribute__((ext_vector_type(4)));
typedef float f16v __attribute__((ext_vector_type(16)));
typedef __bf16 bf8 __attribute__((ext_vector_type(8)));
typedef __bf16 bf2 __attribute__((ext_vector_type(2)));
#define SB() __builtin_amdgcn_sched_barrier(0)

constexpr int L_ = 2048, D_ = 512, N_ = 128, F_ = 1792;
constexpr u32 O_XU = 1u << 20, O_HP = 5u << 20, O_WT = 7u << 20, O_W2T = 11u << 20, O_CS = 13u << 20, O_HM = 14u << 20, WS_USED = 22u << 20;

struct Params { const u16 *x, *lar, *aim, *cr, *ci, *ldt, *dsk, *w1, *w3, *w2; unsigned char* ws; u16* out; };

DI float lo_f(u32 v) { return __uint_as_float(v << 16); }
DI float hi_f(u32 v) { return __uint_as_float(v & 0xffff0000u); }
DI float bf2f(u16 v) { return __uint_as_float((u32)v << 16); }
DI u32 cvtpk(float a, float b) { f2 v = {a, b}; bf2 r = __builtin_convertvector(v, bf2); return __builtin_bit_cast(u32, r); }
DI float rbf(float a) { return lo_f(cvtpk(a, a)); }
template <int C> DI float dppf(float v) { return __int_as_float(__builtin_amdgcn_mov_dpp(__float_as_int(v), C, 0xf, 0xf, true)); }
DI float xsum32(float v) { u32 a = __float_as_uint(v); auto r = __builtin_amdgcn_permlane32_swap(a, a, false, false); u32 r0 = r[0], r1 = r[1]; return __uint_as_float(r0) + __uint_as_float(r1); }
DI float xsum16(float v) { u32 a = __float_as_uint(v); auto r = __builtin_amdgcn_permlane16_swap(a, a, false, false); u32 r0 = r[0], r1 = r[1]; return __uint_as_float(r0) + __uint_as_float(r1); }
DI float wave_sum(float v) { v += dppf<0x128>(v); v += dppf<0x124>(v); v += dppf<0x122>(v); v += dppf<0x121>(v); v = xsum16(v); return xsum32(v); }
DI f4 mfma16(v4u a, v4u b, f4 c) { return __builtin_amdgcn_mfma_f32_16x16x32_bf16(__builtin_bit_cast(bf8, a), __builtin_bit_cast(bf8, b), c, 0, 0, 0); }
DI f16v mfma32(v4u a, v4u b, f16v c) { return __builtin_amdgcn_mfma_f32_32x32x16_bf16(__builtin_bit_cast(bf8, a), __builtin_bit_cast(bf8, b), c, 0, 0, 0); }
DI float dot2(u32 a, u32 b, float c) { return __builtin_amdgcn_fdot2_f32_bf16(__builtin_bit_cast(bf2, a), __builtin_bit_cast(bf2, b), c, false); }
DI f2 cmul(f2 x, f2 e) { f2 en = {-e.y, e.x}; f2 t = x.yy * en; return __builtin_elementwise_fma(x.xx, e, t); }
DI f2 cexp_rev(float re_l2, float rev) { float m = __builtin_amdgcn_exp2f(re_l2); float fr = __builtin_amdgcn_fractf(rev); f2 r = {m * __builtin_amdgcn_cosf(fr), m * __builtin_amdgcn_sinf(fr)}; return r; }
DI void lbar() { asm volatile("s_waitcnt lgkmcnt(0)" ::: "memory"); __builtin_amdgcn_s_barrier(); asm volatile("" ::: "memory"); }
DI void glds16(const void* src, u32 dst) { u32 keep;
  asm volatile("s_mov_b32 %0, m0\n\ts_mov_b32 m0, %2\n\ts_nop 0\n\tglobal_load_lds_dwordx4 %1, off\n\ts_mov_b32 m0, %0" : "=&s"(keep) : "v"(src), "s"(dst) : "memory"); }
typedef __amdgpu_buffer_rsrc_t rsrc_t;
constexpr int AUX = 16;
DI v4u ld16(rsrc_t r, u32 v, u32 s) { return __builtin_amdgcn_raw_buffer_load_b128(r, v, s, AUX); }
DI u32 ld4(rsrc_t r, u32 v, u32 s) { return __builtin_amdgcn_raw_buffer_load_b32(r, v, s, AUX); }
DI void st16(v4u d, rsrc_t r, u32 v, u32 s) { __builtin_amdgcn_raw_buffer_store_b128(d, r, v, s, AUX); }
template <class T> DI LAS T* lp(u32 off) { return (LAS T*)(size_t)off; }

DI void ln_row(v4u r, float& mb, float& rs) {
  float v[8] = {lo_f(r.x), hi_f(r.x), lo_f(r.y), hi_f(r.y), lo_f(r.z), hi_f(r.z), lo_f(r.w), hi_f(r.w)};
  float s = ((v[0] + v[1]) + (v[2] + v[3])) + ((v[4] + v[5]) + (v[6] + v[7]));
  float m = wave_sum(s) * (1.f / D_);
  float q = 0.f;
#pragma unroll
  for (int i = 0; i < 8; i++) { float d = v[i] - m; q = fmaf(d, d, q); }
  float var = rbf(wave_sum(q) * (1.f / D_));
  mb = rbf(m);
  rs = __builtin_amdgcn_rcpf(rbf(__builtin_amdgcn_sqrtf(rbf(var + 1e-5f))));
}
DI u32 norm2(u32 x, float mb, float rs) { return cvtpk((lo_f(x) - mb) * rs, (hi_f(x) - mb) * rs); }

DI void phaseA(const Params& p, rsrc_t rs, int b, int tid, int lane, int w) {
  const GAS v4u* xr = (const GAS v4u*)p.x + (size_t)(8 * b + 2 * w) * 64 + lane;
  v4u r0 = xr[0], r1 = xr[64];
  float m0, s0, m1, s1; ln_row(r0, m0, s0); ln_row(r1, m1, s1);
#pragma unroll
  for (int j = 0; j < 4; j++) {
    u32 x0 = r0[j], x1 = r1[j];
    v4u g = {x0, norm2(x0, m0, s0), x1, norm2(x1, m1, s1)};
    u32 s = 16 * lane + 4 * j + w; s ^= (lane & 7);
    *lp<v4u>(s * 16) = g;
  }
  lbar();
#pragma unroll
  for (int i = 0; i < 4; i++) { u32 s = i * 256 + tid; u32 sp = s ^ ((s >> 4) & 7); v4u g = *lp<v4u>(sp * 16); st16(g, rs, O_XU + b * 16384 + s * 16, 0); }
}
DI u32 pk2(u16 a, u16 b) { return (u32)a | ((u32)b << 16); }
DI void tilingW(const Params& p, rsrc_t rs, int b, int tid, int lane, int w) {
  const int rho = lane & 31, h = lane >> 5;
  if (b < 224) {
    const int T = b >> 1; const int r15 = rho & 15; const int fl = (r15 & 3) | ((r15 & 4) << 1) | ((r15 & 8) >> 1);
    const GAS u16* src = (const GAS u16*)(rho < 16 ? p.w1 : p.w3) + 16 * T + fl;
    float cs = 0.f;
#pragma unroll
    for (int i = 0; i < 4; i++) { const int s = (b & 1) * 16 + w * 4 + i; const GAS u16* q = src + (size_t)(16 * s + 8 * h) * F_;
      v4u g = {pk2(q[0], q[F_]), pk2(q[2 * F_], q[3 * F_]), pk2(q[4 * F_], q[5 * F_]), pk2(q[6 * F_], q[7 * F_])};
      cs = dot2(g.x, 0x3f803f80u, cs); cs = dot2(g.y, 0x3f803f80u, cs); cs = dot2(g.z, 0x3f803f80u, cs); cs = dot2(g.w, 0x3f803f80u, cs);
      st16(g, rs, O_WT + (T * 32 + s) * 1024 + lane * 16, 0); }
    cs = xsum32(cs);
    *lp<float>(16384 + (w * 64 + lane) * 4) = cs;
    lbar();
    if (tid < 32) { float t = *lp<float>(16384 + tid * 4) + *lp<float>(16384 + 256 + tid * 4) + *lp<float>(16384 + 512 + tid * 4) + *lp<float>(16384 + 768 + tid * 4);
      const int hh = (tid >> 2) & 1, reg = (tid & 3) | ((tid >> 3) << 2);
      __builtin_amdgcn_raw_buffer_store_b32(__float_as_uint(t), rs, O_CS + ((T * 2 + (b & 1)) * 32 + hh * 16 + reg) * 4, 0, AUX); }
  }
#pragma unroll
  for (int i = 0; i < 2; i++) { const int e = w + 4 * i; if (e < 7) { const int ks = 7 * (b >> 4) + e, ds = b & 15;
      const GAS u16* q = (const GAS u16*)p.w2 + (size_t)(16 * ks + 8 * h) * D_ + 32 * ds + rho;
      v4u g = {pk2(q[0], q[D_]), pk2(q[2 * D_], q[3 * D_]), pk2(q[4 * D_], q[5 * D_]), pk2(q[6 * D_], q[7 * D_])};
      st16(g, rs, O_W2T + (ds * 112 + ks) * 1024 + lane * 16, 0); } }
}

constexpr u32 FROW = 544, FCH = 53 * FROW;
constexpr u32 LF = 0;
constexpr u32 LKC = 57664, KCH = 1296;
constexpr u32 LKR = 60288, KRC = 4160, KRCH = 4 * KRC;
constexpr u32 LU = 93568, UCH = 4576;
constexpr u32 LY = 102720;

DI void kgen(const Params& p, int pair, int tid, int lane, int w, v4u (&A)[64]) {
  const int ch = w >> 1, d = 2 * pair + ch, n = tid & 127;
  const float L2E = 1.4426950408889634f;
  const float dt = rbf(__builtin_amdgcn_exp2f(bf2f(p.ldt[d]) * L2E));
  const float ar = -rbf(__builtin_amdgcn_exp2f(bf2f(p.lar[d * N_ + n]) * L2E)) * dt * L2E;
  const float ai = bf2f(p.aim[d * N_ + n]) * dt * 0.15915494309189535f;
  f2 X0 = {bf2f(p.cr[d * N_ + n]) * dt * 2.f, bf2f(p.ci[d * N_ + n]) * dt * 2.f};
  const f2 E1 = cexp_rev(ar, ai), E8c = cexp_rev(8.f * ar, -8.f * ai), E256 = cexp_rev(256.f * ar, 256.f * ai);
  const u32 fb = LF + ch * FCH + n * 4;
  auto put = [&](int row, f2 v) { *lp<u32>(fb + row * FROW) = cvtpk(v.x, v.y); };
  { f2 X = X0; put(0, X);
#pragma unroll
    for (int k = 1; k < 8; k++) { X = cmul(X, E1); put(k, X); }
    const f2 E2 = cmul(E1, E1), E4 = cmul(E2, E2);
    f2 Z = cmul(X0, E256); put(8, Z); X = Z;
#pragma unroll
    for (int k = 1; k < 4; k++) { X = cmul(X, E2); put(8 + k, X); }
    Z = cmul(Z, E256); put(12, Z); put(13, cmul(Z, E4));
    Z = cmul(Z, E256); put(14, Z); put(15, cmul(Z, E4));
#pragma unroll
    for (int k = 0; k < 5; k++) { Z = cmul(Z, E256); put(16 + k, Z); }
    f2 Y = {1.f, 0.f}; put(21, Y); Y = E8c; put(22, Y);
#pragma unroll
    for (int k = 2; k < 32; k++) { Y = cmul(Y, E8c); put(21 + k, Y); }
  }
  if (tid < 2) *lp<u32>(LKC + tid * KCH + 642 * 2) = 0u;
  if (tid < 16) *lp<v4u>(LKR + (tid >> 3) * KRCH + ((tid >> 1) & 3) * KRC + 4096 + (tid & 1) * 16) = v4u{0, 0, 0, 0};
  if (tid < 60) *lp<v4u>(LU + (tid / 30) * UCH + (tid % 30) * 16) = v4u{0, 0, 0, 0};
  lbar();
  { const int bt = w & 1, r = lane & 15, g = lane >> 4;
    const u32 ab = LF + ch * FCH + r * FROW + g * 16, bb = ab + (21 + 16 * bt) * FROW;
    f4 d0 = {0, 0, 0, 0}, d1 = {0, 0, 0, 0};
#pragma unroll
    for (int ks = 0; ks < 8; ks++) { v4u a0 = *lp<v4u>(ab + ks * 64), a1 = *lp<v4u>(ab + 16 * FROW + ks * 64), bq = *lp<v4u>(bb + ks * 64);
      d0 = mfma16(a0, bq, d0); d1 = mfma16(a1, bq, d1); }
    const int j = 16 * bt + r;
    const u32 kb = LKC + ch * KCH;
    const int n0 = g < 2 ? 8 * j + 4 * g : (g == 2 ? 256 + 4 * j : 384 + 2 * j), st2 = g == 3 ? 64 : 2;
    const u32 p01 = cvtpk(d0[0], d0[1]), p23 = cvtpk(d0[2], d0[3]);
    *lp<u32>(kb + n0 * 2) = p01; *lp<u32>(kb + (n0 + st2) * 2) = p23;
    const u32 q01 = cvtpk(d1[0], d1[1]), q23 = cvtpk(d1[2], d1[3]);
    if (g == 0) { *lp<u16>(kb + (512 + j) * 2) = (u16)q01; *lp<u16>(kb + (544 + j) * 2) = (u16)(q01 >> 16); *lp<u16>(kb + (576 + j) * 2) = (u16)q23; *lp<u16>(kb + (608 + j) * 2) = (u16)(q23 >> 16); }
    if (g == 1 && j == 0) *lp<u16>(kb + 640 * 2) = (u16)q01;
  }
  lbar();
  { const u32 kb = LKC + ch * KCH; u32 P0[10];
#pragma unroll
    for (int k = 0; k < 10; k++) { u32 e[2];
#pragma unroll
      for (int z = 0; z < 2; z++) { const int lag = 2047 - (16 * n + 2 * k + z); const float lf = (float)lag; const int nb = (int)(cvtpk(lf, lf) & 0xffffu) - 0x4280;
        const int idx = lag < 0 ? 642 : (lag < 128 ? lag : nb); e[z] = *lp<u16>(kb + idx * 2); }
      P0[k] = e[0] | (e[1] << 16); }
    u32 P1[9];
#pragma unroll
    for (int k = 0; k < 9; k++) P1[k] = __builtin_amdgcn_alignbit(P0[k + 1], P0[k], 16);
    const u32 rb = LKR + ch * KRCH + n * 32;
    *lp<v4u>(rb) = v4u{P0[0], P0[1], P0[2], P0[3]}; *lp<v4u>(rb + 16) = v4u{P0[4], P0[5], P0[6], P0[7]};
    *lp<v4u>(rb + KRC) = v4u{P1[0], P1[1], P1[2], P1[3]}; *lp<v4u>(rb + KRC + 16) = v4u{P1[4], P1[5], P1[6], P1[7]};
    *lp<v4u>(rb + 2 * KRC) = v4u{P0[1], P0[2], P0[3], P0[4]}; *lp<v4u>(rb + 2 * KRC + 16) = v4u{P0[5], P0[6], P0[7], P0[8]};
    *lp<v4u>(rb + 3 * KRC) = v4u{P1[1], P1[2], P1[3], P1[4]}; *lp<v4u>(rb + 3 * KRC + 16) = v4u{P1[5], P1[6], P1[7], P1[8]};
  }
  lbar();
  { const int r = lane & 15, kg = lane >> 4; const int sg = (3 - r) & 3; const int e63 = 2031 - r + 8 * kg - 32 * 63 - sg;
    const u32 ab = LKR + ch * KRCH + sg * KRC + 2 * e63;
#pragma unroll
    for (int Q = 0; Q < 64; Q++) { v2u lo = *lp<v2u>(ab + (63 - Q) * 64), hi = *lp<v2u>(ab + (63 - Q) * 64 + 8); A[Q] = v4u{lo.x, lo.y, hi.x, hi.y}; }
  }
}
template <int PAR> DI void conv_mfma(u32 bb, const v4u (&A)[64], f4 (&acc)[4]) {
  constexpr int CT[2][4] = {{0, 3, 4, 7}, {1, 2, 5, 6}};
  constexpr int DMAX = 8 * CT[PAR][3], ND = DMAX + 8;
#pragma unroll
  for (int i = 0; i < 4; i++) acc[i] = f4{0, 0, 0, 0};
#pragma unroll
  for (int it = 0; it < ND; it++) { const int dl = (it & 1) ? DMAX - (it >> 1) : -7 + (it >> 1);
    v4u bq = *lp<v4u>(bb + (dl + 7) * 64);
#pragma unroll
    for (int i = 0; i < 4; i++) { const int Q = 8 * CT[PAR][i] - dl; if (Q >= 0) acc[i] = mfma16(A[Q], bq, acc[i]); }
  }
}
DI void phaseConv(const Params& p, rsrc_t rs, int pair, int tid, int lane, int w) {
  v4u A[64];
  v4u xu[2][2];
#pragma unroll
  for (int k = 0; k < 2; k++) { const u32 o = O_XU + (64 * w + 32 * k + (lane >> 1)) * 16384 + pair * 64 + (lane & 1) * 32; xu[k][0] = ld16(rs, o, 0); xu[k][1] = ld16(rs, o + 16, 0); }
  const u32 dsk = *(const GAS u32*)(p.dsk + 2 * pair);
  kgen(p, pair, tid, lane, w, A);
  const int tq = 128 * w + lane;
#pragma unroll
  for (int k = 0; k < 2; k++) { const u32 o = LU + 480 + (tq + 64 * k) * 8;
    const u32 u0 = xu[k][0].y, u1 = xu[k][0].w, u2 = xu[k][1].y, u3 = xu[k][1].w;
    *lp<v2u>(o) = v2u{__builtin_amdgcn_perm(u1, u0, 0x05040100u), __builtin_amdgcn_perm(u3, u2, 0x05040100u)};
    *lp<v2u>(o + UCH) = v2u{__builtin_amdgcn_perm(u1, u0, 0x07060302u), __builtin_amdgcn_perm(u3, u2, 0x07060302u)}; }
  lbar();
  f4 acc[4]; const int ch = w >> 1, r = lane & 15, g = lane >> 4;
  const u32 bb = LU + ch * UCH + 32 * r + 16 * g;
  const u32 yb = LY + ch * 4096 + 32 * r + 8 * g;
  if (w & 1) { conv_mfma<1>(bb, A, acc); constexpr int CT[4] = {1, 2, 5, 6};
#pragma unroll
    for (int i = 0; i < 4; i++) *lp<v2u>(yb + CT[i] * 512) = v2u{cvtpk(acc[i][0], acc[i][1]), cvtpk(acc[i][2], acc[i][3])};
  } else { conv_mfma<0>(bb, A, acc); constexpr int CT[4] = {0, 3, 4, 7};
#pragma unroll
    for (int i = 0; i < 4; i++) *lp<v2u>(yb + CT[i] * 512) = v2u{cvtpk(acc[i][0], acc[i][1]), cvtpk(acc[i][2], acc[i][3])};
  }
  lbar();
  const float D0 = lo_f(dsk), D1 = hi_f(dsk);
#pragma unroll
  for (int k = 0; k < 2; k++) { const v2u y0 = *lp<v2u>(LY + (tq + 64 * k) * 8), y1 = *lp<v2u>(LY + 4096 + (tq + 64 * k) * 8);
    const u32 xs[4] = {xu[k][0].x, xu[k][0].z, xu[k][1].x, xu[k][1].z}, us[4] = {xu[k][0].y, xu[k][0].w, xu[k][1].y, xu[k][1].w};
    const float ya[4] = {lo_f(y0.x), hi_f(y0.x), lo_f(y0.y), hi_f(y0.y)}, yc[4] = {lo_f(y1.x), hi_f(y1.x), lo_f(y1.y), hi_f(y1.y)};
    v4u o;
#pragma unroll
    for (int t = 0; t < 4; t++) o[t] = cvtpk(lo_f(xs[t]) + fmaf(lo_f(us[t]), D0, ya[t]), hi_f(xs[t]) + fmaf(hi_f(us[t]), D1, yc[t]));
    st16(o, rs, O_HP + pair * 8192 + (tq + 64 * k) * 16, 0); }
}

DI void dma_tile(const unsigned char* wt, int tile, int slot, int lane, int w) {
#pragma unroll
  for (int i = 0; i < 8; i++) glds16(wt + (size_t)tile * 32768 + (w * 8 + i) * 1024 + lane * 16, slot * 32768 + (w * 8 + i) * 1024);
}
DI void phaseG1(const Params& p, rsrc_t rs, int tt, int c, int tid, int lane, int w) {
  const unsigned char* wt = p.ws + O_WT + (size_t)c * 7 * 32768;
  dma_tile(wt, 0, 0, lane, w);
  v4u B[32];
  const int h = lane >> 5;
  { const u32 o = O_HP + (4 * h + ((lane >> 4) & 1) + 2 * ((lane >> 3) & 1)) * 8192 + (tt * 128 + w * 32 + 4 * (lane & 7)) * 4;
#pragma unroll
    for (int s = 0; s < 32; s++) B[s] = ld16(rs, o, s * 65536); }
  SB();
  dma_tile(wt, 1, 1, lane, w); dma_tile(wt, 2, 2, lane, w); dma_tile(wt, 3, 3, lane, w); dma_tile(wt, 4, 4, lane, w);
  SB();
  float sum = 0.f, sq = 0.f;
#pragma unroll
  for (int s = 0; s < 32; s++) {
    auto r0 = __builtin_amdgcn_permlane16_swap(B[s].x, B[s].y, false, false); auto r1 = __builtin_amdgcn_permlane16_swap(B[s].z, B[s].w, false, false);
    const u32 a0 = r0[0], a1 = r0[1], a2 = r1[0], a3 = r1[1];
    v4u t; t.x = __builtin_amdgcn_update_dpp(a0, a2, 0x128, 0xf, 0xc, false); t.z = __builtin_amdgcn_update_dpp(a2, a0, 0x128, 0xf, 0x3, false);
    t.y = __builtin_amdgcn_update_dpp(a1, a3, 0x128, 0xf, 0xc, false); t.w = __builtin_amdgcn_update_dpp(a3, a1, 0x128, 0xf, 0x3, false);
    B[s] = t;
#pragma unroll
    for (int j = 0; j < 4; j++) { const u32 e = t[j]; sum = dot2(e, 0x3f803f80u, sum); sq = dot2(e, e, sq); }
  }
  sum = xsum32(sum); sq = xsum32(sq);
  const float m = sum * (1.f / D_); const float var = rbf(fmaxf(sq * (1.f / D_) - m * m, 0.f));
  const float r = __builtin_amdgcn_rcpf(rbf(__builtin_amdgcn_sqrtf(rbf(var + 1e-5f))));
  const float nm = -rbf(m), c1 = -r * 1.4426950408889634f, r2 = r * r;
  asm volatile("s_waitcnt vmcnt(0)" ::: "memory"); __builtin_amdgcn_s_barrier(); asm volatile("" ::: "memory");
#pragma unroll
  for (int T = 0; T < 7; T++) {
    const int slot = T % 5;
    if (T == 5) { asm volatile("s_waitcnt vmcnt(0)" ::: "memory"); __builtin_amdgcn_s_barrier(); asm volatile("" ::: "memory"); }
    f4 cs[4];
#pragma unroll
    for (int k = 0; k < 4; k++) { const u32 o = O_CS + (c * 7 + T) * 256 + h * 64 + k * 16; v4u a = ld16(rs, o, 0), b2 = ld16(rs, o + 128, 0);
      cs[k] = __builtin_bit_cast(f4, a) + __builtin_bit_cast(f4, b2); }
    f16v acc = {};
#pragma unroll
    for (int s = 0; s < 32; s++) { v4u a = *lp<v4u>(slot * 32768 + s * 1024 + lane * 16); acc = mfma32(a, B[s], acc); }
    if (T < 2) { lbar(); dma_tile(wt, 5 + T, T, lane, w); }
    float o[8];
#pragma unroll
    for (int e = 0; e < 8; e++) { const float G = fmaf(nm, cs[e >> 2][e & 3], acc[e]), U = fmaf(nm, cs[2 + (e >> 2)][e & 3], acc[8 + e]);
      const float ex = __builtin_amdgcn_exp2f(G * c1); o[e] = G * U * r2 * __builtin_amdgcn_rcpf(1.f + ex); }
    st16(v4u{cvtpk(o[0], o[1]), cvtpk(o[2], o[3]), cvtpk(o[4], o[5]), cvtpk(o[6], o[7])}, rs, O_HM + ((tt * 4 + w) * 112 + c * 7 + T) * 1024 + lane * 16, 0);
  }
}

DI void phaseG2(const Params& p, rsrc_t rs, int t2, int n2, int tid, int lane, int w) {
  const int ts0 = w & 1, ds0 = w >> 1, hh = lane >> 5, t = lane & 31;
  const int tau = 4 * (t & 7) + ((t >> 4) & 1) + 2 * ((t >> 3) & 1);
  const int token = 64 * t2 + 32 * ts0 + tau, d0 = 64 * n2 + 32 * ds0;
  u32 hres[8];
#pragma unroll
  for (int k = 0; k < 8; k++) hres[k] = ld4(rs, O_HP + (d0 / 2 + 2 * hh) * 8192 + token * 4, ((k >> 1) * 4 + (k & 1)) * 8192);
  f16v acc[4] = {};
  const u32 wo = O_W2T + (28 * w) * 1024 + lane * 16, ho = O_HM + (28 * w) * 1024 + lane * 16;
  const u32 w0 = wo + ((2 * n2 + ds0) * 112) * 1024, w1 = wo + ((2 * n2 + (ds0 ^ 1)) * 112) * 1024;
  const u32 h0 = ho + ((2 * t2 + ts0) * 112) * 1024, h1 = ho + ((2 * t2 + (ts0 ^ 1)) * 112) * 1024;
  v4u W[2][28], H[2][28];
#pragma unroll
  for (int k = 0; k < 28; k++) { W[0][k] = ld16(rs, w0, k * 1024); H[0][k] = ld16(rs, h0, k * 1024); W[1][k] = ld16(rs, w1, k * 1024); H[1][k] = ld16(rs, h1, k * 1024); if (k == 13) SB(); }
#pragma unroll
  for (int k = 0; k < 28; k++) {
    acc[0] = mfma32(W[0][k], H[0][k], acc[0]); acc[1] = mfma32(W[0][k], H[1][k], acc[1]);
    acc[2] = mfma32(W[1][k], H[0][k], acc[2]); acc[3] = mfma32(W[1][k], H[1][k], acc[3]); }
#pragma unroll
  for (int i = 1; i < 4; i++)
#pragma unroll
    for (int q = 0; q < 4; q++) *lp<f4>((((w ^ i) * 3 + (i - 1)) * 4 + q) * 1024 + lane * 16) = f4{acc[i][4 * q], acc[i][4 * q + 1], acc[i][4 * q + 2], acc[i][4 * q + 3]};
  lbar();
  v2u o2[4];
#pragma unroll
  for (int q = 0; q < 4; q++) { f4 v = {acc[0][4 * q], acc[0][4 * q + 1], acc[0][4 * q + 2], acc[0][4 * q + 3]};
#pragma unroll
    for (int i = 0; i < 3; i++) v += *lp<f4>(((w * 3 + i) * 4 + q) * 1024 + lane * 16);
    o2[q] = v2u{cvtpk(lo_f(hres[2 * q]) + v[0], hi_f(hres[2 * q]) + v[1]), cvtpk(lo_f(hres[2 * q + 1]) + v[2], hi_f(hres[2 * q + 1]) + v[3])}; }
  GAS unsigned char* ob = (GAS unsigned char*)p.out + ((size_t)token * D_ + d0) * 2 + hh * 16;
#pragma unroll
  for (int k = 0; k < 4; k += 2) { auto rx = __builtin_amdgcn_permlane32_swap(o2[k].x, o2[k + 1].x, false, false); auto ry = __builtin_amdgcn_permlane32_swap(o2[k].y, o2[k + 1].y, false, false);
    const u32 ax = rx[0], bx = rx[1], ay = ry[0], by = ry[1];
    *(GAS v4u*)(ob + 16 * k) = v4u{ax, ay, bx, by}; }
}

template <int PH> __global__ void __launch_bounds__(256, 1) mega(Params p) {
  extern __shared__ __attribute__((aligned(16))) char lds_[];
  const int b = blockIdx.x, tid = threadIdx.x, lane = tid & 63, w = __builtin_amdgcn_readfirstlane(tid >> 6);
  const int xcd = b & 7, j = b >> 3;
  const rsrc_t rs = __builtin_amdgcn_make_buffer_rsrc(p.ws, 0, WS_USED, 0x00020000);
  if (PH == 0) { phaseA(p, rs, b, tid, lane, w); tilingW(p, rs, b, tid, lane, w); }
  if (PH == 1) phaseConv(p, rs, xcd * 32 + j, tid, lane, w);
  if (PH == 2) phaseG1(p, rs, xcd * 2 + (j >> 4), j & 15, tid, lane, w);
  if (PH == 3) phaseG2(p, rs, xcd * 4 + (j >> 3), j & 7, tid, lane, w);
}
#define NEXTRA 0
__global__ void k_empty(int* p) { if (p == nullptr) return; }
extern "C" void kernel_launch(void* const* d_in, const int* in_sizes, int n_in, void* d_out, int out_size, void* d_ws, size_t ws_size, hipStream_t stream) {
  Params p; p.x = (const u16*)d_in[0]; p.lar = (const u16*)d_in[1]; p.aim = (const u16*)d_in[2]; p.cr = (const u16*)d_in[3]; p.ci = (const u16*)d_in[4];
  p.ldt = (const u16*)d_in[5]; p.dsk = (const u16*)d_in[6]; p.w1 = (const u16*)d_in[8]; p.w3 = (const u16*)d_in[9]; p.w2 = (const u16*)d_in[10];
  p.ws = (unsigned char*)d_ws; p.out = (u16*)d_out;
  (void)hipFuncSetAttribute((const void*)mega<1>, hipFuncAttributeMaxDynamicSharedMemorySize, 163840);
  (void)hipFuncSetAttribute((const void*)mega<2>, hipFuncAttributeMaxDynamicSharedMemorySize, 163840);
  mega<0><<<256, 256, 32768, stream>>>(p);
  mega<1><<<256, 256, 163840, stream>>>(p);
  mega<2><<<256, 256, 163840, stream>>>(p);
  for (int i = 0; i < NEXTRA; i++) k_empty<<<256, 256, 0, stream>>>((int*)d_ws);
  mega<3><<<256, 256, 49152, stream>>>(p);
}
```

```cpp
#include <hip/hip_runtime.h>
typedef unsigned short u16;
#define WS_BASE(sz) ((size_t)0)
struct Params { const void *x, *lar, *aim, *cr, *ci, *ldt, *dsk, *w1, *w3, *w2; void* ws; void* out; };
__global__ void __launch_bounds__(256, 1) s4d_swiglu_fused(Params p) {
  asm volatile(
".p2align 5\n"
"s_load_dwordx16 s[36:51], s[0:1], 0\n"
"s_load_dwordx8 s[52:59], s[0:1], 64\n"
"s_getpc_b64 s[60:61]\n"
"s_lshr_b32 s34, s2, 3\n"
"v_and_b32_e64 v61, 7, v0\n"
"v_lshl_add_u32 v61, s34, 3, v61\n"
"v_lshlrev_b32 v61, 7, v61\n"
"v_min_u32 v61, 25052, v61\n"
"global_load_dword v62, v61, s[60:61]\n"
"v_and_b32 v1, 63, v0\n"
"v_lshlrev_b32 v2, 4, v1\n"
"v_lshlrev_b32 v3, 4, v0\n"
"v_readfirstlane_b32 s3, v0\n"
"s_and_b32 s34, s2, 7\n"
"s_lshr_b32 s35, s2, 3\n"
"s_lshr_b32 s3, s3, 6\n"
"s_lshl_b32 s28, s34, 5\n"
"s_add_u32 s28, s28, s35\n"
"s_mov_b32 s10, 2097152\n"
"s_mov_b32 s11, 0x20000\n"
"v_lshlrev_b32 v20, 1, v0\n"
"s_lshl_b32 s8, s28, 9\n"
"v_add_u32 v20, s8, v20\n"
"s_lshl_b32 s85, s2, 13\n"
"s_lshl_b32 s69, s3, 11\n"
"s_add_u32 s12, s85, s69\n"
"s_add_u32 s85, s12, 1048576\n"
"s_lshl_b32 s13, s28, 2\n"
"v_mov_b32 v27, s13\n"
"v_mov_b32 v48, 0\n"
"s_waitcnt lgkmcnt(0)\n"
"s_mov_b32 s8, s36\n"
"s_and_b32 s9, s37, 0xffff\n"
"buffer_load_dwordx4 v[4:7], v2, s[8:11], s12 offen\n"
"buffer_load_dwordx4 v[8:11], v2, s[8:11], s12 offen offset:1024\n"
"s_nop 0\n"
"global_load_ushort v12, v20, s[38:39]\n"
"global_load_ushort v13, v20, s[40:41]\n"
"global_load_ushort v14, v20, s[42:43]\n"
"global_load_ushort v15, v20, s[44:45]\n"
"global_load_dword v44, v27, s[46:47]\n"
"global_load_dword v45, v27, s[48:49]\n"
"global_load_dwordx2 v[46:47], v48, s[56:57]\n"
"s_and_b32 s34, s2, 7\n"
"s_lshr_b32 s35, s2, 3\n"
"s_mov_b32 s6, 18874368\n"
"s_mov_b32 s7, 0x20000\n"
"s_mov_b32 s26, 2097152\n"
"s_mov_b32 s27, 0x20000\n"
"s_cmp_lt_u32 s2, 224\n"
"s_cselect_b32 s86, 0, 2130706432\n"
"v_lshrrev_b32 v24, 3, v1\n"
"v_and_b32 v25, 7, v1\n"
"s_lshr_b32 s34, s3, 1\n"
"s_and_b32 s35, s3, 1\n"
"s_lshl_b32 s35, s35, 3\n"
"s_add_u32 s35, s35, 148480\n"
"v_lshl_add_u32 v31, v1, 3, s34\n"
"v_add_u32 v36, 0, v31\n"
"v_xor_b32_e64 v36, v36, v25\n"
"v_lshl_add_u32 v36, v36, 4, s35\n"
"v_add_u32 v37, 2, v31\n"
"v_xor_b32 v37, v37, v25\n"
"v_lshl_add_u32 v37, v37, 4, s35\n"
"v_add_u32 v38, 4, v31\n"
"v_xor_b32 v38, v38, v25\n"
"v_lshl_add_u32 v38, v38, 4, s35\n"
"v_add_u32 v39, 6, v31\n"
"v_xor_b32 v39, v39, v25\n"
"v_lshl_add_u32 v39, v39, 4, s35\n"
"v_xor_b32_e64 v35, v1, v24\n"
"s_add_u32 s34, s69, 148480\n"
"v_lshl_add_u32 v35, v35, 4, s34\n"
"v_mov_b32 v34, 2130706432\n"
"s_lshl_b32 s34, s2, 6\n"
"s_add_u32 s82, s34, 102400\n"
"v_and_b32 v33, 2, v1\n"
"s_lshl_b32 s35, s3, 4\n"
"s_add_u32 s35, s35, 156672\n"
"v_lshl_add_u32 v33, v33, 2, s35\n"
"v_and_b32 v32, 48, v2\n"
"v_add_u32 v32, 156672, v32\n"
"s_mov_b32 s92, 0xaaaaaaaa\n"
"s_mov_b32 s93, 0xaaaaaaaa\n"
"s_mov_b32 s96, 0xcccccccc\n"
"s_mov_b32 s97, 0xcccccccc\n"
"s_movk_i32 s16, 3584\n"
"s_lshr_b32 s34, s2, 3\n"
"s_and_b32 s35, s2, 7\n"
"s_cmp_eq_u32 s3, 3\n"
"s_cselect_b32 s16, 1024, s16\n"
"s_cselect_b32 s17, s34, s35\n"
"s_cselect_b32 s18, s35, s34\n"
"v_mul_u32_u24 v26, s16, v24\n"
"v_lshl_add_u32 v26, v25, 4, v26\n"
"v_add_u32 v26, s86, v26\n"
"s_lshl_b32 s16, s16, 3\n"
"s_mul_i32 s17, s17, s16\n"
"s_lshl_b32 s17, s17, 3\n"
"s_lshl_b32 s18, s18, 7\n"
"s_add_u32 s17, s17, s18\n"
"s_cmp_lt_u32 s2, 224\n"
"s_cselect_b32 s17, s17, 0\n"
"s_lshl_b32 s18, s3, 13\n"
"s_add_u32 s18, s18, -8192\n"
"v_cmp_gt_u32 vcc, 4, v0\n"
"v_lshlrev_b32 v126, 6, v0\n"
"s_lshl_b32 s34, s2, 6\n"
"v_cndmask_b32 v124, v34, v3, vcc\n"
"v_cmp_gt_u32 vcc, 8, v0\n"
"s_add_u32 s22, s34, 4096\n"
"s_lshl_b32 s34, s2, 7\n"
"v_cndmask_b32 v125, v34, v3, vcc\n"
"s_add_u32 s23, s34, 36864\n"
"s_add_u32 s88, s34, 69632\n"
"v_mov_b32 v56, 0\n"
"v_mov_b32 v57, 0\n"
"v_mov_b32 v58, 0\n"
"v_mov_b32 v59, 0\n"
"s_mov_b32 s4, s56\n"
"s_and_b32 s5, s57, 0xffff\n"
"s_mov_b32 s24, s58\n"
"s_and_b32 s25, s59, 0xffff\n"
"s_cmp_eq_u32 s3, 3\n"
"s_cselect_b32 s12, s54, s50\n"
"s_cselect_b32 s13, s55, s51\n"
"s_cmp_eq_u32 s3, 2\n"
"s_cselect_b32 s12, s52, s12\n"
"s_cselect_b32 s13, s53, s13\n"
"s_and_b32 s13, s13, 0xffff\n"
"s_mov_b32 s14, 1835008\n"
"s_mov_b32 s15, 0x20000\n"
"s_barrier\n"
"s_cmp_eq_u32 s3, 0\n"
"s_cbranch_scc1 L_noraw\n"
"s_mov_b32 m0, s18\n"
"s_add_u32 s18, s18, 1024\n"
"buffer_load_dwordx4 v26, s[12:15], s17 offen lds\n"
"s_add_u32 s17, s17, s16\n"
"s_mov_b32 m0, s18\n"
"s_add_u32 s18, s18, 1024\n"
"buffer_load_dwordx4 v26, s[12:15], s17 offen lds\n"
"s_add_u32 s17, s17, s16\n"
"s_mov_b32 m0, s18\n"
"s_add_u32 s18, s18, 1024\n"
"buffer_load_dwordx4 v26, s[12:15], s17 offen lds\n"
"s_add_u32 s17, s17, s16\n"
"s_mov_b32 m0, s18\n"
"s_add_u32 s18, s18, 1024\n"
"buffer_load_dwordx4 v26, s[12:15], s17 offen lds\n"
"s_add_u32 s17, s17, s16\n"
"s_mov_b32 m0, s18\n"
"s_add_u32 s18, s18, 1024\n"
"buffer_load_dwordx4 v26, s[12:15], s17 offen lds\n"
"s_add_u32 s17, s17, s16\n"
"s_mov_b32 m0, s18\n"
"s_add_u32 s18, s18, 1024\n"
"buffer_load_dwordx4 v26, s[12:15], s17 offen lds\n"
"s_add_u32 s17, s17, s16\n"
"s_mov_b32 m0, s18\n"
"s_add_u32 s18, s18, 1024\n"
"buffer_load_dwordx4 v26, s[12:15], s17 offen lds\n"
"s_add_u32 s17, s17, s16\n"
"s_mov_b32 m0, s18\n"
"s_add_u32 s18, s18, 1024\n"
"buffer_load_dwordx4 v26, s[12:15], s17 offen lds\n"
"s_add_u32 s17, s17, s16\n"
"L_noraw:\n"
"buffer_store_dwordx4 v[56:59], v125, s[4:7], s88 offen sc1\n"
"s_lshr_b32 s36, s3, 1\n"
"s_and_b32 s37, s3, 1\n"
"s_mul_i32 s34, s36, 28832\n"
"s_lshl_b32 s35, s37, 8\n"
"s_add_u32 s34, s34, s35\n"
"s_add_u32 s40, s34, 32768\n"
"s_and_b32 s34, s2, 7\n"
"s_lshr_b32 s35, s2, 3\n"
"s_lshr_b32 s29, s34, 1\n"
"s_lshl_b32 s29, s29, 2\n"
"s_lshr_b32 s30, s35, 3\n"
"s_add_u32 s29, s29, s30\n"
"s_and_b32 s30, s34, 1\n"
"s_lshl_b32 s30, s30, 3\n"
"s_and_b32 s33, s35, 7\n"
"s_add_u32 s30, s30, s33\n"
"s_lshr_b32 s31, s28, 3\n"
"s_lshl_b32 s84, s3, 19\n"
"s_lshl_b32 s34, s28, 7\n"
"s_add_u32 s84, s84, s34\n"
"s_add_u32 s84, s84, 3145728\n"
"s_add_u32 s89, s84, 262144\n"
"s_lshl_b32 s34, s29, 2\n"
"s_add_u32 s34, s34, s3\n"
"s_lshl_b32 s79, s34, 4\n"
"s_add_u32 s79, s79, s30\n"
"s_lshl_b32 s79, s79, 9\n"
"s_add_u32 s79, s79, 131072\n"
"s_mul_i32 s34, s34, 112\n"
"s_mul_i32 s35, s30, 7\n"
"s_add_u32 s34, s34, s35\n"
"s_lshl_b32 s34, s34, 10\n"
"s_add_u32 s67, s34, 11534336\n"
"s_add_u32 s68, s67, 4096\n"
"s_lshl_b32 s70, s3, 13\n"
"s_mul_i32 s34, s30, 229376\n"
"s_add_u32 s34, s34, s70\n"
"s_add_u32 s20, s34, 5242880\n"
"s_add_u32 s21, s20, 4096\n"
"s_lshl_b32 s34, s3, 19\n"
"s_lshl_b32 s35, s28, 5\n"
"s_add_u32 s34, s34, s35\n"
"s_add_u32 s80, s34, 1048576\n"
"s_add_u32 s81, s80, 262144\n"
"s_mov_b32 s87, 0x05040100\n"
"v_min_u32 v30, 29, v1\n"
"v_lshlrev_b32_e64 v30, 4, v30\n"
"s_and_b32 s34, s3, 1\n"
"s_mul_i32 s34, s34, 4576\n"
"s_add_u32 s34, s34, 131072\n"
"v_add_u32_e64 v30, s34, v30\n"
"ds_write_b128 v30, v[56:59]\n"
"v_bfe_u32 v31, v1, 2, 3\n"
"v_mul_u32_u24 v31, 4160, v31\n"
"v_and_b32_e64 v30, 3, v1\n"
"v_lshl_add_u32 v31, v30, 4, v31\n"
"v_add_u32 v31, 101872, v31\n"
"ds_write_b128 v31, v[56:59]\n"
"v_and_b32_e64 v30, 1, v1\n"
"v_mul_u32_u24 v30, 1536, v30\n"
"ds_write_b64 v30, v[56:57] offset:24576\n"
"v_add_u32_e64 v60, s86, v2\n"
"s_cmp_eq_u32 s3, 0\n"
"s_cbranch_scc0 L_wx\n"
"s_waitcnt vmcnt(8)\n"
"L_wx:\n"
"s_waitcnt vmcnt(16)\n"
"ds_write2_b32 v36, v4, v8 offset0:0 offset1:1\n"
"ds_write2_b32 v37, v5, v9 offset0:0 offset1:1\n"
"ds_write2_b32 v38, v6, v10 offset0:0 offset1:1\n"
"ds_write2_b32 v39, v7, v11 offset0:0 offset1:1\n"
"v_mov_b32 v40, 0\n"
"v_mov_b32 v41, 0\n"
"v_mov_b32 v42, 0\n"
"v_mov_b32_e64 v43, 0\n"
"v_dot2c_f32_bf16 v40, 0x3f803f80, v4\n"
"v_dot2c_f32_bf16 v41, v4, v4\n"
"v_dot2c_f32_bf16 v42, 0x3f803f80, v8\n"
"v_dot2c_f32_bf16 v43, v8, v8\n"
"v_dot2c_f32_bf16 v40, 0x3f803f80, v5\n"
"v_dot2c_f32_bf16 v41, v5, v5\n"
"v_dot2c_f32_bf16 v42, 0x3f803f80, v9\n"
"v_dot2c_f32_bf16 v43, v9, v9\n"
"v_dot2c_f32_bf16 v40, 0x3f803f80, v6\n"
"v_dot2c_f32_bf16 v41, v6, v6\n"
"v_dot2c_f32_bf16 v42, 0x3f803f80, v10\n"
"v_dot2c_f32_bf16 v43, v10, v10\n"
"v_dot2c_f32_bf16 v40, 0x3f803f80, v7\n"
"v_dot2c_f32_bf16 v41, v7, v7\n"
"v_dot2c_f32_bf16 v42, 0x3f803f80, v11\n"
"v_dot2c_f32_bf16 v43, v11, v11\n"
"s_waitcnt lgkmcnt(0)\n"
"s_barrier\n"
"s_nop 0\n"
"ds_read_b128 v[128:131], v35 offset:0\n"
"ds_read_b128 v[132:135], v35 offset:1024\n"
"v_add_f32_dpp v40, v40, v40 quad_perm:[1,0,3,2] row_mask:0xf bank_mask:0xf\n"
"v_add_f32_dpp v41, v41, v41 quad_perm:[1,0,3,2] row_mask:0xf bank_mask:0xf\n"
"v_add_f32_dpp v42, v42, v42 quad_perm:[1,0,3,2] row_mask:0xf bank_mask:0xf\n"
"v_add_f32_dpp v43, v43, v43 quad_perm:[1,0,3,2] row_mask:0xf bank_mask:0xf\n"
"v_cndmask_b32 v40, v40, v41, s[92:93]\n"
"v_cndmask_b32 v42, v42, v43, s[92:93]\n"
"s_nop 0\n"
"v_add_f32_dpp v40, v40, v40 quad_perm:[2,3,0,1] row_mask:0xf bank_mask:0xf\n"
"v_add_f32_dpp v42, v42, v42 quad_perm:[2,3,0,1] row_mask:0xf bank_mask:0xf\n"
"v_cndmask_b32 v40, v40, v42, s[96:97]\n"
"s_waitcnt lgkmcnt(1)\n"
"buffer_store_dwordx4 v[128:131], v2, s[4:7], s85 offen sc1\n"
"v_add_f32_dpp v40, v40, v40 row_ror:4 row_mask:0xf bank_mask:0xf\n"
"s_waitcnt lgkmcnt(0)\n"
"buffer_store_dwordx4 v[132:135], v2, s[4:7], s85 offen offset:1024 sc1\n"
"v_add_f32_dpp v40, v40, v40 row_ror:8 row_mask:0xf bank_mask:0xf\n"
"v_mov_b32 v41, v40\n"
"s_lshr_b32 s34, s2, 3\n"
"s_lshl_b32 s34, s34, 2\n"
"v_permlane16_swap_b32 v40, v41\n"
"v_add_f32 v40, v40, v41\n"
"v_mov_b32 v41, v40\n"
"s_add_u32 s34, s34, s3\n"
"s_and_b32 s35, s2, 7\n"
"v_permlane32_swap_b32 v40, v41\n"
"v_add_f32_e64 v40, v40, v41\n"
"v_mul_f32 v40, 0x3b000000, v40\n"
"s_lshl_b32 s16, s34, 10\n"
"s_lshl_b32 s38, s35, 7\n"
"v_mov_b32_dpp v41, v40 quad_perm:[1,1,3,3] row_mask:0xf bank_mask:0xf\n"
"v_mov_b32_dpp v40, v40 quad_perm:[0,0,2,2] row_mask:0xf bank_mask:0xf\n"
"v_fma_f32 v41, -v40, v40, v41\n"
"v_max_f32_e64 v41, 0, v41\n"
"v_cvt_pk_bf16_f32 v40, v40, v41\n"
"v_and_b32 v41, 0xffff0000, v40\n"
"v_lshlrev_b32_e64 v40, 16, v40\n"
"v_add_f32 v41, 0x3727c5ac, v41\n"
"v_cvt_pk_bf16_f32 v41, v41, v41\n"
"v_lshlrev_b32 v41, 16, v41\n"
"v_sqrt_f32 v41, v41\n"
"s_add_u32 s16, s16, s38\n"
"v_cvt_pk_bf16_f32 v41, v41, v41\n"
"v_lshlrev_b32 v41, 16, v41\n"
"v_rcp_f32 v42, v41\n"
"s_add_u32 s16, s16, 11403264\n"
"v_mul_f32 v43, -v40, v42\n"
"ds_write_b64 v33, v[42:43]\n"
"s_waitcnt lgkmcnt(0)\n"
"s_barrier\n"
"ds_read_b128 v[40:43], v32\n"
"s_waitcnt lgkmcnt(0)\n"
"buffer_store_dwordx4 v[40:43], v124, s[4:7], s82 offen sc1\n"
"s_cmp_eq_u32 s3, 0\n"
"s_cbranch_scc0 L_wp\n"
"s_waitcnt vmcnt(4)\n"
"L_wp:\n"
"s_waitcnt vmcnt(12)\n"
"v_readfirstlane_b32 s64, v44\n"
"v_readfirstlane_b32 s60, v46\n"
"v_readfirstlane_b32 s61, v47\n"
"v_readfirstlane_b32 s65, v45\n"
"s_lshr_b32 s39, s3, 1\n"
"s_lshl_b32 s39, s39, 4\n"
"s_lshr_b32 s39, s64, s39\n"
"s_lshl_b32 s39, s39, 16\n"
"v_mov_b32 v40, 0x3fb8aa3b\n"
"v_mul_f32 v41, s39, v40\n"
"v_lshlrev_b32 v12, 16, v12\n"
"v_mul_f32 v12, v12, v40\n"
"v_exp_f32 v41, v41\n"
"v_exp_f32 v12, v12\n"
"v_lshlrev_b32 v13, 16, v13\n"
"v_lshlrev_b32 v14, 16, v14\n"
"v_lshlrev_b32 v15, 16, v15\n"
"v_cvt_pk_bf16_f32 v41, v41, v12\n"
"v_and_b32 v12, 0xffff0000, v41\n"
"v_lshlrev_b32 v41, 16, v41\n"
"v_mul_f32 v12, v12, v41\n"
"v_mul_f32 v13, v13, v41\n"
"v_add_f32 v41, v41, v41\n"
"v_mul_f32 v12, 0xbfb8aa3b, v12\n"
"v_mul_f32 v13, 0.15915494, v13\n"
"v_mul_f32 v14, v14, v41\n"
"v_mul_f32_e64 v15, v15, v41\n"
"v_mul_f32 v64, 0x41000000, v12\n"
"v_mul_f32 v65, 0xc1000000, v13\n"
"v_mul_f32 v66, 0x43800000, v12\n"
"v_mul_f32 v67, 0x43800000, v13\n"
"v_exp_f32 v64, v64\n"
"v_fract_f32 v65, v65\n"
"v_exp_f32 v52, v12\n"
"v_fract_f32 v53, v13\n"
"v_exp_f32 v66, v66\n"
"v_fract_f32 v67, v67\n"
"v_cos_f32 v44, v65\n"
"v_sin_f32 v45, v65\n"
"v_cos_f32 v42, v53\n"
"v_sin_f32 v43, v53\n"
"v_cos_f32 v46, v67\n"
"v_sin_f32 v47, v67\n"
"v_pk_mul_f32 v[44:45], v[44:45], v[64:65] op_sel_hi:[1,0]\n"
"v_pk_mul_f32 v[42:43], v[42:43], v[52:53] op_sel_hi:[1,0]\n"
"v_pk_mul_f32 v[46:47], v[46:47], v[66:67] op_sel_hi:[1,0]\n"
"s_mov_b32 m0, s40\n"
"v_mov_b32 v91, 0x3f80\n"
"s_nop 0\n"
"v_cvt_pk_bf16_f32 v92, v14, v15\n"
"ds_write_addtid_b32 v91 offset:11424\n"
"ds_write_addtid_b32 v92 offset:0\n"
"v_cvt_pk_bf16_f32 v95, v44, v45\n"
"v_pk_mul_f32 v[72:73], v[14:15], v[42:43] op_sel:[1,1] op_sel_hi:[1,0] neg_lo:[0,1]\n"
"ds_write_addtid_b32 v95 offset:11968\n"
"v_pk_mul_f32 v[74:75], v[44:45], v[44:45] op_sel:[1,1] op_sel_hi:[1,0] neg_lo:[0,1]\n"
"v_pk_fma_f32 v[64:65], v[14:15], v[42:43], v[72:73] op_sel_hi:[0,1,1]\n"
"v_pk_fma_f32 v[68:69], v[44:45], v[44:45], v[74:75] op_sel_hi:[0,1,1]\n"
"v_cvt_pk_bf16_f32 v93, v64, v65\n"
"v_cvt_pk_bf16_f32 v94, v68, v69\n"
"ds_write_addtid_b32 v94 offset:12512\n"
"ds_write_addtid_b32 v93 offset:544\n"
"v_pk_mul_f32 v[74:75], v[68:69], v[44:45] op_sel:[1,1] op_sel_hi:[1,0] neg_lo:[0,1]\n"
"v_pk_mul_f32 v[72:73], v[64:65], v[42:43] op_sel:[1,1] op_sel_hi:[1,0] neg_lo:[0,1]\n"
"v_pk_fma_f32 v[68:69], v[68:69], v[44:45], v[74:75] op_sel_hi:[0,1,1]\n"
"v_cvt_pk_bf16_f32 v95, v68, v69\n"
"v_pk_fma_f32 v[64:65], v[64:65], v[42:43], v[72:73] op_sel_hi:[0,1,1]\n"
"ds_write_addtid_b32 v95 offset:13056\n"
"v_cvt_pk_bf16_f32 v92, v64, v65\n"
"v_pk_mul_f32 v[74:75], v[68:69], v[44:45] op_sel:[1,1] op_sel_hi:[1,0] neg_lo:[0,1]\n"
"ds_write_addtid_b32 v92 offset:1088\n"
"v_pk_fma_f32 v[68:69], v[68:69], v[44:45], v[74:75] op_sel_hi:[0,1,1]\n"
"v_cvt_pk_bf16_f32 v94, v68, v69\n"
"v_pk_mul_f32 v[72:73], v[64:65], v[42:43] op_sel:[1,1] op_sel_hi:[1,0] neg_lo:[0,1]\n"
"ds_write_addtid_b32 v94 offset:13600\n"
"v_pk_fma_f32 v[64:65], v[64:65], v[42:43], v[72:73] op_sel_hi:[0,1,1]\n"
"v_pk_mul_f32 v[74:75], v[68:69], v[44:45] op_sel:[1,1] op_sel_hi:[1,0] neg_lo:[0,1]\n"
"v_pk_fma_f32 v[68:69], v[68:69], v[44:45], v[74:75] op_sel_hi:[0,1,1]\n"
"s_lshl_b32 s17, s34, 15\n"
"s_lshl_b32 s38, s35, 12\n"
"s_add_u32 s17, s17, s38\n"
"s_add_u32 s17, s17, 5242880\n"
"s_mul_i32 s35, s35, 224\n"
"s_add_u32 s34, s34, s35\n"
"s_lshl_b32 s34, s34, 10\n"
"s_add_u32 s18, s34, 9437184\n"
"s_add_u32 s19, s18, 114688\n"
"s_add_u32 s60, s60, 1\n"
"s_addc_u32 s61, s61, 0\n"
"s_mul_i32 s62, s60, 0x7f4a7c15\n"
"s_mul_hi_u32 s63, s60, 0x7f4a7c15\n"
"s_mul_i32 s34, s60, 0x9e3779b9\n"
"s_add_u32 s63, s63, s34\n"
"s_mul_i32 s34, s61, 0x7f4a7c15\n"
"s_add_u32 s63, s63, s34\n"
"v_mov_b32 v16, s62\n"
"v_mov_b32 v17, s63\n"
"v_mov_b32 v18, s62\n"
"v_mov_b32_e64 v19, s63\n"
"v_bfe_u32 v30, v1, 2, 2\n"
"v_lshrrev_b32 v31, 5, v1\n"
"v_lshl_add_u32 v30, v31, 3, v30\n"
"v_lshlrev_b32 v30, 7, v30\n"
"v_and_b32_e64 v31, 1, v1\n"
"v_lshl_add_u32 v30, v31, 4, v30\n"
"v_bfe_u32 v31, v1, 1, 1\n"
"v_lshl_add_u32 v30, v31, 3, v30\n"
"v_bfe_u32 v31, v1, 4, 1\n"
"v_lshl_add_u32 v109, v31, 13, v30\n"
"v_lshl_add_u32 v110, v31, 5, v30\n"
"s_lshl_b32 s34, s3, 5\n"
"v_add_u32_e64 v109, s34, v109\n"
"s_lshl_b32 s34, s3, 11\n"
"s_add_u32 s34, s34, 16384\n"
"v_add_u32 v110, s34, v110\n"
"v_bfe_u32 v30, v1, 2, 1\n"
"v_lshlrev_b32 v30, 2, v30\n"
"v_and_b32_e64 v31, 3, v1\n"
"v_lshl_add_u32 v30, v31, 4, v30\n"
"v_bfe_u32 v31, v1, 3, 1\n"
"v_lshl_add_u32 v30, v31, 6, v30\n"
"v_bfe_u32 v31, v1, 4, 1\n"
"v_lshl_add_u32 v111, v31, 3, v30\n"
"s_mul_i32 s34, s36, 28832\n"
"s_add_u32 s34, s34, 32768\n"
"v_and_b32 v55, 15, v1\n"
"v_lshrrev_b32 v29, 4, v1\n"
"v_mul_u32_u24 v30, 544, v55\n"
"v_lshl_add_u32 v30, v29, 4, v30\n"
"v_add_u32_e64 v112, s34, v30\n"
"s_mul_i32 s35, s37, 8704\n"
"s_add_u32 s35, s35, 11424\n"
"v_add_u32_e64 v113, s35, v112\n"
"s_mul_i32 s34, s36, 16640\n"
"s_lshl_b32 s35, s37, 8\n"
"s_add_u32 s41, s34, s35\n"
"s_add_u32 s41, s41, 48636\n"
"s_lshl_b32 s35, s37, 6\n"
"s_sub_u32 s34, s34, s35\n"
"s_add_u32 s34, s34, 97856\n"
"v_sub_u32 v30, 3, v55\n"
"v_and_b32 v30, 3, v30\n"
"v_lshlrev_b32 v31, 3, v29\n"
"v_sub_u32 v31, v31, v55\n"
"v_sub_u32 v31, v31, v30\n"
"v_add_u32 v31, 15, v31\n"
"v_mul_u32_u24 v30, 4160, v30\n"
"v_lshl_add_u32 v30, v31, 1, v30\n"
"v_add_u32 v120, s34, v30\n"
"v_lshlrev_b32 v30, 5, v55\n"
"v_lshl_add_u32 v31, v29, 3, v30\n"
"v_lshl_add_u32 v30, v29, 4, v30\n"
"s_mul_i32 s34, s36, 4576\n"
"s_sub_u32 s34, s34, s35\n"
"s_add_u32 s34, s34, 131136\n"
"v_add_u32 v122, s34, v30\n"
"s_lshl_b32 s34, s3, 12\n"
"s_add_u32 s34, s34, 140224\n"
"v_add_u32 v123, s34, v31\n"
"s_lshl_b32 s35, s37, 4\n"
"v_add_u32 v55, s35, v55\n"
"s_mul_i32 s34, s36, 1536\n"
"s_add_u32 s34, s34, 24584\n"
"s_sub_u32 s42, s34, 0x96800000\n"
"v_lshlrev_b32 v28, 4, v55\n"
"v_lshl_add_u32 v28, v29, 3, v28\n"
"v_lshlrev_b32 v30, 3, v55\n"
"v_add_u32 v30, 512, v30\n"
"v_cmp_eq_u32 vcc, 2, v29\n"
"v_lshlrev_b32 v31, 2, v55\n"
"v_add_u32 v31, 768, v31\n"
"v_cndmask_b32 v28, v28, v30, vcc\n"
"v_cmp_eq_u32 vcc, 3, v29\n"
"v_mov_b32 v62, 4\n"
"v_mov_b32 v30, 128\n"
"v_cndmask_b32 v28, v28, v31, vcc\n"
"v_cndmask_b32 v62, v62, v30, vcc\n"
"v_add_u32 v114, s34, v28\n"
"v_add_u32_e64 v115, v114, v62\n"
"v_lshl_add_u32 v28, v55, 1, s34\n"
"v_add_u32 v28, 1024, v28\n"
"s_add_u32 s35, s34, 1288\n"
"v_cmp_eq_u32 vcc, 0, v29\n"
"v_mov_b32 v30, s35\n"
"s_add_u32 s35, s34, 1280\n"
"v_cndmask_b32_e64 v117, v30, v28, vcc\n"
"v_lshl_add_u32 v30, v55, 2, v29\n"
"v_cmp_eq_u32 vcc, 1, v30\n"
"v_mov_b32 v30, s35\n"
"v_lshrrev_b32 v31, 1, v1\n"
"v_cndmask_b32 v116, v117, v30, vcc\n"
"v_lshlrev_b32 v31, 13, v31\n"
"v_and_b32 v30, 1, v1\n"
"v_lshl_add_u32 v119, v30, 4, v31\n"
"s_lshl_b32 s34, s3, 10\n"
"s_add_u32 s34, s34, 131552\n"
"v_lshl_add_u32 v121, v1, 3, s34\n"
"v_lshlrev_b32 v30, 15, v24\n"
"v_lshl_add_u32 v107, v25, 4, v30\n"
"v_add_u32_e64 v111, s86, v111\n"
"s_waitcnt vmcnt(0)\n"
"s_barrier\n"
"buffer_store_dwordx4 v[16:19], v124, s[4:7], s22 offen sc1\n"
"ds_read_b64_tr_b16 v[128:129], v109 offset:0\n"
"v_cvt_pk_bf16_f32 v93, v64, v65\n"
"ds_read_b64_tr_b16 v[130:131], v109 offset:512\n"
"v_cvt_pk_bf16_f32 v95, v68, v69\n"
"ds_read_b64_tr_b16 v[132:133], v109 offset:2048\n"
"ds_write_addtid_b32 v93 offset:1632\n"
"ds_read_b64_tr_b16 v[134:135], v109 offset:2560\n"
"ds_write_addtid_b32 v95 offset:14144\n"
"ds_read_b64_tr_b16 v[136:137], v109 offset:4096\n"
"v_pk_mul_f32 v[74:75], v[68:69], v[44:45] op_sel:[1,1] op_sel_hi:[1,0] neg_lo:[0,1]\n"
"ds_read_b64_tr_b16 v[138:139], v109 offset:4608\n"
"v_pk_mul_f32 v[72:73], v[64:65], v[42:43] op_sel:[1,1] op_sel_hi:[1,0] neg_lo:[0,1]\n"
"ds_read_b64_tr_b16 v[140:141], v109 offset:6144\n"
"v_pk_fma_f32 v[68:69], v[68:69], v[44:45], v[74:75] op_sel_hi:[0,1,1]\n"
"ds_read_b64_tr_b16 v[142:143], v109 offset:6656\n"
"v_pk_fma_f32 v[64:65], v[64:65], v[42:43], v[72:73] op_sel_hi:[0,1,1]\n"
"s_waitcnt lgkmcnt(8)\n"
"buffer_store_dwordx4 v[128:131], v60, s[4:7], s17 offen offset:0 sc1\n"
"v_cvt_pk_bf16_f32 v94, v68, v69\n"
"v_dot2c_f32_bf16 v56, 0x3f803f80, v128\n"
"s_nop 0\n"
"ds_write_addtid_b32 v94 offset:14688\n"
"v_dot2c_f32_bf16 v57, 0x3f803f80, v129\n"
"v_cvt_pk_bf16_f32 v92, v64, v65\n"
"v_dot2c_f32_bf16 v56, 0x3f803f80, v130\n"
"v_pk_mul_f32 v[74:75], v[68:69], v[44:45] op_sel:[1,1] op_sel_hi:[1,0] neg_lo:[0,1]\n"
"v_dot2c_f32_bf16 v57, 0x3f803f80, v131\n"
"ds_write_addtid_b32 v92 offset:2176\n"
"v_pk_fma_f32 v[68:69], v[68:69], v[44:45], v[74:75] op_sel_hi:[0,1,1]\n"
"v_pk_mul_f32 v[72:73], v[64:65], v[42:43] op_sel:[1,1] op_sel_hi:[1,0] neg_lo:[0,1]\n"
"v_cvt_pk_bf16_f32 v95, v68, v69\n"
"ds_write_addtid_b32 v95 offset:15232\n"
"v_pk_fma_f32 v[64:65], v[64:65], v[42:43], v[72:73] op_sel_hi:[0,1,1]\n"
"s_waitcnt lgkmcnt(8)\n"
"buffer_store_dwordx4 v[132:135], v60, s[4:7], s17 offen offset:1024 sc1\n"
"v_pk_mul_f32 v[74:75], v[68:69], v[44:45] op_sel:[1,1] op_sel_hi:[1,0] neg_lo:[0,1]\n"
"v_dot2c_f32_bf16 v56, 0x3f803f80, v132\n"
"s_nop 0\n"
"v_cvt_pk_bf16_f32 v93, v64, v65\n"
"v_dot2c_f32_bf16 v57, 0x3f803f80, v133\n"
"v_pk_fma_f32 v[68:69], v[68:69], v[44:45], v[74:75] op_sel_hi:[0,1,1]\n"
"v_dot2c_f32_bf16 v56, 0x3f803f80, v134\n"
"v_cvt_pk_bf16_f32 v94, v68, v69\n"
"v_dot2c_f32_bf16 v57, 0x3f803f80, v135\n"
"ds_write_addtid_b32 v93 offset:2720\n"
"ds_write_addtid_b32 v94 offset:15776\n"
"v_pk_mul_f32 v[72:73], v[64:65], v[42:43] op_sel:[1,1] op_sel_hi:[1,0] neg_lo:[0,1]\n"
"v_pk_mul_f32 v[74:75], v[68:69], v[44:45] op_sel:[1,1] op_sel_hi:[1,0] neg_lo:[0,1]\n"
"v_pk_fma_f32 v[68:69], v[68:69], v[44:45], v[74:75] op_sel_hi:[0,1,1]\n"
"v_pk_fma_f32 v[64:65], v[64:65], v[42:43], v[72:73] op_sel_hi:[0,1,1]\n"
"s_waitcnt lgkmcnt(7)\n"
"buffer_store_dwordx4 v[136:139], v60, s[4:7], s17 offen offset:2048 sc1\n"
"v_cvt_pk_bf16_f32 v95, v68, v69\n"
"v_dot2c_f32_bf16 v56, 0x3f803f80, v136\n"
"s_nop 0\n"
"v_cvt_pk_bf16_f32 v92, v64, v65\n"
"v_dot2c_f32_bf16 v57, 0x3f803f80, v137\n"
"ds_write_addtid_b32 v95 offset:16320\n"
"v_dot2c_f32_bf16 v56, 0x3f803f80, v138\n"
"v_pk_mul_f32 v[74:75], v[68:69], v[44:45] op_sel:[1,1] op_sel_hi:[1,0] neg_lo:[0,1]\n"
"v_dot2c_f32_bf16 v57, 0x3f803f80, v139\n"
"ds_write_addtid_b32 v92 offset:3264\n"
"v_pk_fma_f32 v[68:69], v[68:69], v[44:45], v[74:75] op_sel_hi:[0,1,1]\n"
"v_pk_mul_f32 v[72:73], v[64:65], v[42:43] op_sel:[1,1] op_sel_hi:[1,0] neg_lo:[0,1]\n"
"v_cvt_pk_bf16_f32 v94, v68, v69\n"
"v_pk_fma_f32 v[64:65], v[64:65], v[42:43], v[72:73] op_sel_hi:[0,1,1]\n"
"ds_write_addtid_b32 v94 offset:16864\n"
"s_waitcnt lgkmcnt(8)\n"
"buffer_store_dwordx4 v[140:143], v60, s[4:7], s17 offen offset:3072 sc1\n"
"v_pk_mul_f32 v[74:75], v[68:69], v[44:45] op_sel:[1,1] op_sel_hi:[1,0] neg_lo:[0,1]\n"
"v_dot2c_f32_bf16 v56, 0x3f803f80, v140\n"
"s_nop 0\n"
"v_cvt_pk_bf16_f32 v93, v64, v65\n"
"v_dot2c_f32_bf16 v57, 0x3f803f80, v141\n"
"v_pk_fma_f32 v[68:69], v[68:69], v[44:45], v[74:75] op_sel_hi:[0,1,1]\n"
"v_dot2c_f32_bf16 v56, 0x3f803f80, v142\n"
"ds_write_addtid_b32 v93 offset:3808\n"
"v_dot2c_f32_bf16 v57, 0x3f803f80, v143\n"
"v_cvt_pk_bf16_f32 v95, v68, v69\n"
"ds_write_addtid_b32 v95 offset:17408\n"
"v_pk_mul_f32 v[72:73], v[42:43], v[42:43] op_sel:[1,1] op_sel_hi:[1,0] neg_lo:[0,1]\n"
"v_pk_mul_f32 v[74:75], v[68:69], v[44:45] op_sel:[1,1] op_sel_hi:[1,0] neg_lo:[0,1]\n"
"v_pk_fma_f32 v[48:49], v[42:43], v[42:43], v[72:73] op_sel_hi:[0,1,1]\n"
"v_pk_fma_f32 v[68:69], v[68:69], v[44:45], v[74:75] op_sel_hi:[0,1,1]\n"
"v_add_f32 v56, v56, v57\n"
"v_cvt_pk_bf16_f32 v94, v68, v69\n"
"v_mov_b32 v57, v56\n"
"v_pk_mul_f32 v[72:73], v[14:15], v[46:47] op_sel:[1,1] op_sel_hi:[1,0] neg_lo:[0,1]\n"
"ds_write_addtid_b32 v94 offset:17952\n"
"v_pk_fma_f32 v[66:67], v[14:15], v[46:47], v[72:73] op_sel_hi:[0,1,1]\n"
"v_permlane32_swap_b32 v56, v57\n"
"v_pk_mul_f32 v[74:75], v[68:69], v[44:45] op_sel:[1,1] op_sel_hi:[1,0] neg_lo:[0,1]\n"
"v_add_f32 v56, v56, v57\n"
"v_pk_fma_f32 v[68:69], v[68:69], v[44:45], v[74:75] op_sel_hi:[0,1,1]\n"
"buffer_store_dword v56, v111, s[4:7], s16 offen sc1\n"
"v_cvt_pk_bf16_f32 v92, v66, v67\n"
"v_cvt_pk_bf16_f32 v95, v68, v69\n"
"ds_write_addtid_b32 v92 offset:4352\n"
"ds_write_addtid_b32 v95 offset:18496\n"
"v_pk_mul_f32 v[72:73], v[48:49], v[48:49] op_sel:[1,1] op_sel_hi:[1,0] neg_lo:[0,1]\n"
"v_pk_mul_f32 v[74:75], v[68:69], v[44:45] op_sel:[1,1] op_sel_hi:[1,0] neg_lo:[0,1]\n"
"v_pk_fma_f32 v[68:69], v[68:69], v[44:45], v[74:75] op_sel_hi:[0,1,1]\n"
"v_pk_fma_f32 v[50:51], v[48:49], v[48:49], v[72:73] op_sel_hi:[0,1,1]\n"
"v_cvt_pk_bf16_f32 v94, v68, v69\n"
"v_pk_mul_f32 v[72:73], v[66:67], v[48:49] op_sel:[1,1] op_sel_hi:[1,0] neg_lo:[0,1]\n"
"ds_write_addtid_b32 v94 offset:19040\n"
"v_pk_mul_f32 v[74:75], v[68:69], v[44:45] op_sel:[1,1] op_sel_hi:[1,0] neg_lo:[0,1]\n"
"v_pk_fma_f32 v[64:65], v[66:67], v[48:49], v[72:73] op_sel_hi:[0,1,1]\n"
"v_pk_fma_f32 v[68:69], v[68:69], v[44:45], v[74:75] op_sel_hi:[0,1,1]\n"
"v_cvt_pk_bf16_f32 v93, v64, v65\n"
"v_cvt_pk_bf16_f32 v95, v68, v69\n"
"ds_write_addtid_b32 v95 offset:19584\n"
"ds_write_addtid_b32 v93 offset:4896\n"
"v_pk_mul_f32 v[74:75], v[68:69], v[44:45] op_sel:[1,1] op_sel_hi:[1,0] neg_lo:[0,1]\n"
"v_pk_mul_f32 v[72:73], v[64:65], v[48:49] op_sel:[1,1] op_sel_hi:[1,0] neg_lo:[0,1]\n"
"v_pk_fma_f32 v[68:69], v[68:69], v[44:45], v[74:75] op_sel_hi:[0,1,1]\n"
"v_cvt_pk_bf16_f32 v94, v68, v69\n"
"v_pk_fma_f32 v[64:65], v[64:65], v[48:49], v[72:73] op_sel_hi:[0,1,1]\n"
"ds_write_addtid_b32 v94 offset:20128\n"
"v_cvt_pk_bf16_f32 v92, v64, v65\n"
"v_pk_mul_f32 v[74:75], v[68:69], v[44:45] op_sel:[1,1] op_sel_hi:[1,0] neg_lo:[0,1]\n"
"ds_write_addtid_b32 v92 offset:5440\n"
"v_pk_fma_f32 v[68:69], v[68:69], v[44:45], v[74:75] op_sel_hi:[0,1,1]\n"
"v_cvt_pk_bf16_f32 v95, v68, v69\n"
"v_pk_mul_f32 v[72:73], v[64:65], v[48:49] op_sel:[1,1] op_sel_hi:[1,0] neg_lo:[0,1]\n"
"ds_write_addtid_b32 v95 offset:20672\n"
"v_pk_fma_f32 v[64:65], v[64:65], v[48:49], v[72:73] op_sel_hi:[0,1,1]\n"
"v_pk_mul_f32 v[74:75], v[68:69], v[44:45] op_sel:[1,1] op_sel_hi:[1,0] neg_lo:[0,1]\n"
"v_pk_fma_f32 v[68:69], v[68:69], v[44:45], v[74:75] op_sel_hi:[0,1,1]\n"
"v_cvt_pk_bf16_f32 v93, v64, v65\n"
"v_cvt_pk_bf16_f32 v94, v68, v69\n"
"ds_write_addtid_b32 v93 offset:5984\n"
"ds_write_addtid_b32 v94 offset:21216\n"
"v_pk_mul_f32 v[74:75], v[68:69], v[44:45] op_sel:[1,1] op_sel_hi:[1,0] neg_lo:[0,1]\n"
"v_pk_mul_f32 v[72:73], v[66:67], v[46:47] op_sel:[1,1] op_sel_hi:[1,0] neg_lo:[0,1]\n"
"v_pk_fma_f32 v[68:69], v[68:69], v[44:45], v[74:75] op_sel_hi:[0,1,1]\n"
"v_pk_fma_f32 v[66:67], v[66:67], v[46:47], v[72:73] op_sel_hi:[0,1,1]\n"
"v_cvt_pk_bf16_f32 v95, v68, v69\n"
"v_cvt_pk_bf16_f32 v92, v66, v67\n"
"ds_write_addtid_b32 v95 offset:21760\n"
"v_pk_mul_f32 v[74:75], v[68:69], v[44:45] op_sel:[1,1] op_sel_hi:[1,0] neg_lo:[0,1]\n"
"ds_write_addtid_b32 v92 offset:6528\n"
"v_pk_fma_f32 v[68:69], v[68:69], v[44:45], v[74:75] op_sel_hi:[0,1,1]\n"
"v_pk_mul_f32 v[72:73], v[66:67], v[50:51] op_sel:[1,1] op_sel_hi:[1,0] neg_lo:[0,1]\n"
"v_cvt_pk_bf16_f32 v94, v68, v69\n"
"ds_write_addtid_b32 v94 offset:22304\n"
"v_pk_fma_f32 v[64:65], v[66:67], v[50:51], v[72:73] op_sel_hi:[0,1,1]\n"
"v_pk_mul_f32 v[74:75], v[68:69], v[44:45] op_sel:[1,1] op_sel_hi:[1,0] neg_lo:[0,1]\n"
"v_cvt_pk_bf16_f32 v93, v64, v65\n"
"v_pk_fma_f32 v[68:69], v[68:69], v[44:45], v[74:75] op_sel_hi:[0,1,1]\n"
"v_cvt_pk_bf16_f32 v95, v68, v69\n"
"ds_write_addtid_b32 v93 offset:7072\n"
"ds_write_addtid_b32 v95 offset:22848\n"
"v_pk_mul_f32 v[72:73], v[66:67], v[46:47] op_sel:[1,1] op_sel_hi:[1,0] neg_lo:[0,1]\n"
"v_pk_mul_f32 v[74:75], v[68:69], v[44:45] op_sel:[1,1] op_sel_hi:[1,0] neg_lo:[0,1]\n"
"v_pk_fma_f32 v[68:69], v[68:69], v[44:45], v[74:75] op_sel_hi:[0,1,1]\n"
"v_pk_fma_f32 v[66:67], v[66:67], v[46:47], v[72:73] op_sel_hi:[0,1,1]\n"
"v_cvt_pk_bf16_f32 v94, v68, v69\n"
"v_cvt_pk_bf16_f32 v92, v66, v67\n"
"ds_write_addtid_b32 v94 offset:23392\n"
"ds_write_addtid_b32 v92 offset:7616\n"
"v_pk_mul_f32 v[74:75], v[68:69], v[44:45] op_sel:[1,1] op_sel_hi:[1,0] neg_lo:[0,1]\n"
"v_pk_fma_f32 v[68:69], v[68:69], v[44:45], v[74:75] op_sel_hi:[0,1,1]\n"
"v_pk_mul_f32 v[72:73], v[66:67], v[50:51] op_sel:[1,1] op_sel_hi:[1,0] neg_lo:[0,1]\n"
"v_cvt_pk_bf16_f32 v95, v68, v69\n"
"v_pk_fma_f32 v[64:65], v[66:67], v[50:51], v[72:73] op_sel_hi:[0,1,1]\n"
"ds_write_addtid_b32 v95 offset:23936\n"
"v_pk_mul_f32 v[74:75], v[68:69], v[44:45] op_sel:[1,1] op_sel_hi:[1,0] neg_lo:[0,1]\n"
"v_cvt_pk_bf16_f32 v93, v64, v65\n"
"v_pk_fma_f32 v[68:69], v[68:69], v[44:45], v[74:75] op_sel_hi:[0,1,1]\n"
"ds_write_addtid_b32 v93 offset:8160\n"
"v_cvt_pk_bf16_f32 v94, v68, v69\n"
"ds_write_addtid_b32 v94 offset:24480\n"
"v_pk_mul_f32 v[72:73], v[66:67], v[46:47] op_sel:[1,1] op_sel_hi:[1,0] neg_lo:[0,1]\n"
"v_pk_mul_f32 v[74:75], v[68:69], v[44:45] op_sel:[1,1] op_sel_hi:[1,0] neg_lo:[0,1]\n"
"v_pk_fma_f32 v[66:67], v[66:67], v[46:47], v[72:73] op_sel_hi:[0,1,1]\n"
"v_pk_fma_f32 v[68:69], v[68:69], v[44:45], v[74:75] op_sel_hi:[0,1,1]\n"
"v_cvt_pk_bf16_f32 v95, v68, v69\n"
"v_cvt_pk_bf16_f32 v92, v66, v67\n"
"ds_write_addtid_b32 v95 offset:25024\n"
"ds_write_addtid_b32 v92 offset:8704\n"
"v_pk_mul_f32 v[74:75], v[68:69], v[44:45] op_sel:[1,1] op_sel_hi:[1,0] neg_lo:[0,1]\n"
"v_pk_mul_f32 v[72:73], v[66:67], v[46:47] op_sel:[1,1] op_sel_hi:[1,0] neg_lo:[0,1]\n"
"v_pk_fma_f32 v[68:69], v[68:69], v[44:45], v[74:75] op_sel_hi:[0,1,1]\n"
"v_cvt_pk_bf16_f32 v94, v68, v69\n"
"v_pk_fma_f32 v[66:67], v[66:67], v[46:47], v[72:73] op_sel_hi:[0,1,1]\n"
"ds_write_addtid_b32 v94 offset:25568\n"
"v_cvt_pk_bf16_f32 v93, v66, v67\n"
"v_pk_mul_f32 v[74:75], v[68:69], v[44:45] op_sel:[1,1] op_sel_hi:[1,0] neg_lo:[0,1]\n"
"v_pk_fma_f32 v[68:69], v[68:69], v[44:45], v[74:75] op_sel_hi:[0,1,1]\n"
"ds_write_addtid_b32 v93 offset:9248\n"
"v_cvt_pk_bf16_f32 v95, v68, v69\n"
"v_pk_mul_f32 v[72:73], v[66:67], v[46:47] op_sel:[1,1] op_sel_hi:[1,0] neg_lo:[0,1]\n"
"ds_write_addtid_b32 v95 offset:26112\n"
"v_pk_mul_f32 v[74:75], v[68:69], v[44:45] op_sel:[1,1] op_sel_hi:[1,0] neg_lo:[0,1]\n"
"v_pk_fma_f32 v[66:67], v[66:67], v[46:47], v[72:73] op_sel_hi:[0,1,1]\n"
"v_pk_fma_f32 v[68:69], v[68:69], v[44:45], v[74:75] op_sel_hi:[0,1,1]\n"
"v_cvt_pk_bf16_f32 v92, v66, v67\n"
"v_cvt_pk_bf16_f32 v94, v68, v69\n"
"ds_write_addtid_b32 v94 offset:26656\n"
"ds_write_addtid_b32 v92 offset:9792\n"
"v_pk_mul_f32 v[74:75], v[68:69], v[44:45] op_sel:[1,1] op_sel_hi:[1,0] neg_lo:[0,1]\n"
"v_pk_mul_f32 v[72:73], v[66:67], v[46:47] op_sel:[1,1] op_sel_hi:[1,0] neg_lo:[0,1]\n"
"v_pk_fma_f32 v[68:69], v[68:69], v[44:45], v[74:75] op_sel_hi:[0,1,1]\n"
"v_pk_fma_f32 v[66:67], v[66:67], v[46:47], v[72:73] op_sel_hi:[0,1,1]\n"
"v_cvt_pk_bf16_f32 v95, v68, v69\n"
"ds_write_addtid_b32 v95 offset:27200\n"
"v_cvt_pk_bf16_f32 v93, v66, v67\n"
"v_pk_mul_f32 v[74:75], v[68:69], v[44:45] op_sel:[1,1] op_sel_hi:[1,0] neg_lo:[0,1]\n"
"ds_write_addtid_b32 v93 offset:10336\n"
"v_pk_fma_f32 v[68:69], v[68:69], v[44:45], v[74:75] op_sel_hi:[0,1,1]\n"
"v_cvt_pk_bf16_f32 v94, v68, v69\n"
"v_pk_mul_f32 v[72:73], v[66:67], v[46:47] op_sel:[1,1] op_sel_hi:[1,0] neg_lo:[0,1]\n"
"ds_write_addtid_b32 v94 offset:27744\n"
"v_pk_fma_f32 v[66:67], v[66:67], v[46:47], v[72:73] op_sel_hi:[0,1,1]\n"
"v_pk_mul_f32 v[74:75], v[68:69], v[44:45] op_sel:[1,1] op_sel_hi:[1,0] neg_lo:[0,1]\n"
"v_pk_fma_f32 v[68:69], v[68:69], v[44:45], v[74:75] op_sel_hi:[0,1,1]\n"
"v_cvt_pk_bf16_f32 v92, v66, v67\n"
"v_cvt_pk_bf16_f32 v95, v68, v69\n"
"ds_write_addtid_b32 v92 offset:10880\n"
"ds_write_addtid_b32 v95 offset:28288\n"
"s_mov_b32 s83, 4096\n"
"s_waitcnt lgkmcnt(0)\n"
"s_barrier\n"
"ds_read_b128 v[160:163], v112 offset:0\n"
"ds_read_b128 v[168:171], v113 offset:0\n"
"ds_read_b128 v[164:167], v112 offset:8704\n"
"ds_read_b128 v[172:175], v112 offset:64\n"
"ds_read_b128 v[180:183], v113 offset:64\n"
"ds_read_b128 v[176:179], v112 offset:8768\n"
"ds_read_b128 v[184:187], v112 offset:128\n"
"ds_read_b128 v[192:195], v113 offset:128\n"
"ds_read_b128 v[188:191], v112 offset:8832\n"
"ds_read_b128 v[196:199], v112 offset:192\n"
"ds_read_b128 v[204:207], v113 offset:192\n"
"ds_read_b128 v[200:203], v112 offset:8896\n"
"ds_read_b128 v[208:211], v112 offset:256\n"
"ds_read_b128 v[216:219], v113 offset:256\n"
"ds_read_b128 v[212:215], v112 offset:8960\n"
"ds_read_b128 v[220:223], v112 offset:320\n"
"ds_read_b128 v[228:231], v113 offset:320\n"
"ds_read_b128 v[224:227], v112 offset:9024\n"
"ds_read_b128 v[232:235], v112 offset:384\n"
"ds_read_b128 v[240:243], v113 offset:384\n"
"ds_read_b128 v[236:239], v112 offset:9088\n"
"ds_read_b128 v[244:247], v112 offset:448\n"
"ds_read_b128 v[252:255], v113 offset:448\n"
"ds_read_b128 v[248:251], v112 offset:9152\n"
"buffer_load_dwordx2 v[44:45], v126, s[4:7], s83 offen sc1\n"
"v_and_b32 v72, 127, v0\n"
"v_lshlrev_b32_e64 v72, 1, v72\n"
"v_sub_u32 v72, 2047, v72\n"
"v_cvt_f32_i32 v72, v72\n"
"v_add_f32 v73, -1.0, v72\n"
"v_add_f32 v74, -2.0, v72\n"
"v_mul_f32 v75, 0.5, v72\n"
"v_mul_f32 v76, 0.5, v73\n"
"v_mul_f32 v77, 0.5, v74\n"
"v_mul_f32 v78, 0.5, v75\n"
"v_mul_f32 v79, 0.5, v76\n"
"v_mul_f32 v80, 0.5, v77\n"
"v_mul_f32 v81, 0.5, v78\n"
"v_mul_f32 v82, 0.5, v79\n"
"v_mul_f32 v83, 0.5, v80\n"
"v_add_f32 v96, 0x4b400180, v81\n"
"v_add_f32 v97, 0x4b400180, v82\n"
"v_add_f32 v98, 0x4b400180, v83\n"
"v_add_f32 v99, 0x4b400160, v81\n"
"v_add_f32 v100, 0x4b400160, v82\n"
"v_add_f32 v101, 0x4b400160, v83\n"
"v_add_f32 v102, 0x4b400140, v81\n"
"v_add_f32 v103, 0x4b400140, v82\n"
"v_add_f32 v104, 0x4b400140, v83\n"
"v_add_f32 v105, 0x4b400120, v81\n"
"v_add_f32 v128, 0x4b400120, v82\n"
"v_add_f32 v129, 0x4b400120, v83\n"
"v_add_f32 v130, 0x4b400000, v78\n"
"v_add_f32 v131, 0x4b400000, v79\n"
"v_add_f32 v132, 0x4b400000, v80\n"
"s_waitcnt lgkmcnt(15)\n"
"v_mfma_f32_16x16x32_bf16 v[64:67], v[160:163], v[168:171], 0\n"
"v_add_f32 v133, 0x4b3fffc0, v78\n"
"v_mfma_f32_16x16x32_bf16 v[68:71], v[164:167], v[168:171], 0\n"
"v_add_f32 v134, 0x4b3fffc0, v79\n"
"s_waitcnt lgkmcnt(15)\n"
"v_mfma_f32_16x16x32_bf16 v[64:67], v[172:175], v[180:183], v[64:67]\n"
"v_add_f32 v135, 0x4b3fffc0, v80\n"
"v_mfma_f32_16x16x32_bf16 v[68:71], v[176:179], v[180:183], v[68:71]\n"
"v_add_f32 v136, 0x4b3ffd80, v75\n"
"s_waitcnt lgkmcnt(15)\n"
"v_mfma_f32_16x16x32_bf16 v[64:67], v[184:187], v[192:195], v[64:67]\n"
"v_add_f32 v137, 0x4b3ffd80, v76\n"
"v_mfma_f32_16x16x32_bf16 v[68:71], v[188:191], v[192:195], v[68:71]\n"
"v_add_f32 v138, 0x4b3ffd80, v77\n"
"s_waitcnt lgkmcnt(12)\n"
"v_mfma_f32_16x16x32_bf16 v[64:67], v[196:199], v[204:207], v[64:67]\n"
"v_add_f32 v139, 0x4b3ff900, v72\n"
"v_mfma_f32_16x16x32_bf16 v[68:71], v[200:203], v[204:207], v[68:71]\n"
"v_add_f32 v140, 0x4b3ff900, v73\n"
"s_waitcnt lgkmcnt(9)\n"
"v_mfma_f32_16x16x32_bf16 v[64:67], v[208:211], v[216:219], v[64:67]\n"
"v_add_f32 v141, 0x4b3ff900, v74\n"
"v_mfma_f32_16x16x32_bf16 v[68:71], v[212:215], v[216:219], v[68:71]\n"
"v_add_f32 v84, 0x4b3ffa00, v74\n"
"s_waitcnt lgkmcnt(6)\n"
"v_mfma_f32_16x16x32_bf16 v[64:67], v[220:223], v[228:231], v[64:67]\n"
"v_min_u32_e64 v138, v138, v84\n"
"v_mfma_f32_16x16x32_bf16 v[68:71], v[224:227], v[228:231], v[68:71]\n"
"v_lshl_add_u32 v96, v96, 1, s42\n"
"s_waitcnt lgkmcnt(3)\n"
"v_mfma_f32_16x16x32_bf16 v[64:67], v[232:235], v[240:243], v[64:67]\n"
"v_lshl_add_u32 v97, v97, 1, s42\n"
"v_mfma_f32_16x16x32_bf16 v[68:71], v[236:239], v[240:243], v[68:71]\n"
"v_lshl_add_u32 v98, v98, 1, s42\n"
"s_waitcnt lgkmcnt(0)\n"
"v_mfma_f32_16x16x32_bf16 v[64:67], v[244:247], v[252:255], v[64:67]\n"
"v_lshl_add_u32 v99, v99, 1, s42\n"
"v_mfma_f32_16x16x32_bf16 v[68:71], v[248:251], v[252:255], v[68:71]\n"
"v_lshl_add_u32 v100, v100, 1, s42\n"
"v_lshl_add_u32 v101, v101, 1, s42\n"
"v_lshl_add_u32 v102, v102, 1, s42\n"
"v_lshl_add_u32 v103, v103, 1, s42\n"
"v_lshl_add_u32 v104, v104, 1, s42\n"
"v_lshl_add_u32 v105, v105, 1, s42\n"
"v_lshl_add_u32 v128, v128, 1, s42\n"
"v_lshl_add_u32 v129, v129, 1, s42\n"
"v_lshl_add_u32 v130, v130, 1, s42\n"
"v_lshl_add_u32 v131, v131, 1, s42\n"
"v_lshl_add_u32 v132, v132, 1, s42\n"
"v_lshl_add_u32 v133, v133, 1, s42\n"
"v_lshl_add_u32 v134, v134, 1, s42\n"
"v_lshl_add_u32 v135, v135, 1, s42\n"
"v_lshl_add_u32 v136, v136, 1, s42\n"
"v_lshl_add_u32 v137, v137, 1, s42\n"
"v_lshl_add_u32 v138, v138, 1, s42\n"
"v_lshl_add_u32 v139, v139, 1, s42\n"
"v_lshl_add_u32 v140, v140, 1, s42\n"
"v_lshl_add_u32 v141, v141, 1, s42\n"
"v_lshlrev_b32_e64 v47, 5, v1\n"
"s_lshl_b32 s34, s3, 12\n"
"s_add_u32 s82, s34, 102400\n"
"s_mov_b32 m0, s41\n"
"v_cvt_pk_bf16_f32 v64, v64, v65\n"
"v_cvt_pk_bf16_f32 v66, v66, v67\n"
"ds_write_b32 v114, v64\n"
"ds_write_b32 v115, v66\n"
"v_cvt_pk_bf16_f32 v68, v68, v69\n"
"v_cvt_pk_bf16_f32 v70, v70, v71\n"
"ds_write_b16 v116, v68\n"
"ds_write_b16_d16_hi v117, v68 offset:64\n"
"ds_write_b16 v117, v70 offset:128\n"
"ds_write_b16_d16_hi v117, v70 offset:192\n"
"s_waitcnt vmcnt(1)\n"
"s_waitcnt lgkmcnt(0)\n"
"s_barrier\n"
"s_nop 0\n"
"buffer_store_dwordx4 v[16:19], v125, s[4:7], s23 offen sc1\n"
"ds_read_u16 v160, v96\n"
"ds_read_u16 v161, v97\n"
"ds_read_u16 v162, v98\n"
"ds_read_u16 v163, v99\n"
"ds_read_u16 v164, v100\n"
"ds_read_u16 v165, v101\n"
"ds_read_u16 v166, v102\n"
"ds_read_u16 v167, v103\n"
"ds_read_u16 v168, v104\n"
"ds_read_u16 v169, v105\n"
"ds_read_u16 v170, v128\n"
"ds_read_u16 v171, v129\n"
"ds_read_u16 v172, v130\n"
"ds_read_u16 v173, v131\n"
"ds_read_u16 v174, v132\n"
"ds_read_u16 v175, v133\n"
"ds_read_u16 v176, v134\n"
"ds_read_u16 v177, v135\n"
"ds_read_u16 v178, v136\n"
"ds_read_u16 v179, v137\n"
"ds_read_u16 v180, v138\n"
"ds_read_u16 v181, v139\n"
"ds_read_u16 v182, v140\n"
"ds_read_u16 v183, v141\n"
"s_waitcnt vmcnt(1)\n"
"v_cmp_ne_u64_e64 vcc, s[62:63], v[44:45]\n"
"s_cbranch_vccnz L_f1\n"
"L_f1_back:\n"
"buffer_load_dwordx4 v[4:7], v119, s[4:7], s80 offen sc1\n"
"buffer_load_dwordx4 v[20:23], v47, s[4:7], s82 offen offset:0 sc1\n"
"buffer_load_dwordx4 v[24:27], v47, s[4:7], s82 offen offset:16 sc1\n"
"buffer_load_dwordx4 v[8:11], v119, s[4:7], s81 offen sc1\n"
"buffer_load_dwordx4 v[28:31], v47, s[4:7], s82 offen offset:2048 sc1\n"
"buffer_load_dwordx4 v[32:35], v47, s[4:7], s82 offen offset:2064 sc1\n"
"s_waitcnt lgkmcnt(15)\n"
"v_perm_b32 v64, v161, v160, s87\n"
"v_perm_b32 v65, v162, v161, s87\n"
"s_nop 0\n"
"ds_write_addtid_b32 v64 offset:49156\n"
"ds_write_addtid_b32 v65 offset:53316\n"
"ds_write_addtid_b32 v64 offset:57472\n"
"ds_write_addtid_b32 v65 offset:61632\n"
"v_perm_b32 v66, v164, v163, s87\n"
"v_perm_b32 v67, v165, v164, s87\n"
"ds_write_addtid_b32 v66 offset:49668\n"
"ds_write_addtid_b32 v67 offset:53828\n"
"ds_write_addtid_b32 v66 offset:57984\n"
"ds_write_addtid_b32 v67 offset:62144\n"
"s_waitcnt lgkmcnt(15)\n"
"v_perm_b32 v68, v167, v166, s87\n"
"s_nop 0\n"
"v_perm_b32 v69, v168, v167, s87\n"
"ds_write_addtid_b32 v68 offset:50180\n"
"ds_write_addtid_b32 v69 offset:54340\n"
"ds_write_addtid_b32 v68 offset:58496\n"
"ds_write_addtid_b32 v69 offset:62656\n"
"v_perm_b32 v70, v170, v169, s87\n"
"v_perm_b32 v71, v171, v170, s87\n"
"ds_write_addtid_b32 v70 offset:50692\n"
"ds_write_addtid_b32 v71 offset:54852\n"
"ds_write_addtid_b32 v70 offset:59008\n"
"ds_write_addtid_b32 v71 offset:63168\n"
"s_waitcnt lgkmcnt(15)\n"
"s_nop 0\n"
"v_perm_b32 v72, v173, v172, s87\n"
"v_perm_b32 v73, v174, v173, s87\n"
"ds_write_addtid_b32 v72 offset:51204\n"
"ds_write_addtid_b32 v73 offset:55364\n"
"ds_write_addtid_b32 v72 offset:59520\n"
"ds_write_addtid_b32 v73 offset:63680\n"
"v_perm_b32 v74, v176, v175, s87\n"
"v_perm_b32 v75, v177, v176, s87\n"
"ds_write_addtid_b32 v74 offset:51716\n"
"ds_write_addtid_b32 v75 offset:55876\n"
"ds_write_addtid_b32 v74 offset:60032\n"
"ds_write_addtid_b32 v75 offset:64192\n"
"s_waitcnt lgkmcnt(15)\n"
"v_perm_b32 v76, v179, v178, s87\n"
"v_perm_b32 v77, v180, v179, s87\n"
"ds_write_addtid_b32 v76 offset:52228\n"
"s_nop 0\n"
"ds_write_addtid_b32 v77 offset:56388\n"
"ds_write_addtid_b32 v76 offset:60544\n"
"ds_write_addtid_b32 v77 offset:64704\n"
"v_perm_b32 v78, v182, v181, s87\n"
"v_perm_b32 v79, v183, v182, s87\n"
"ds_write_addtid_b32 v78 offset:52740\n"
"ds_write_addtid_b32 v79 offset:56900\n"
"ds_write_addtid_b32 v78 offset:61056\n"
"ds_write_addtid_b32 v79 offset:65216\n"
"s_mul_i32 s34, s30, 7\n"
"s_lshr_b32 s34, s34, 2\n"
"s_lshl_b32 s34, s34, 3\n"
"v_min_u32 v47, 23, v1\n"
"v_add_u32_e64 v47, s34, v47\n"
"v_min_u32 v47, 223, v47\n"
"v_lshlrev_b32_e64 v47, 7, v47\n"
"s_mov_b32 s83, 36864\n"
"s_mul_i32 s38, s30, 7168\n"
"s_add_u32 s38, s38, 11403264\n"
"s_add_u32 s39, s38, 4096\n"
"s_waitcnt lgkmcnt(0)\n"
"s_barrier\n"
"ds_read_b64_tr_b16 v[64:65], v110 offset:0\n"
"ds_read_b64_tr_b16 v[66:67], v110 offset:512\n"
"ds_read_b64_tr_b16 v[68:69], v110 offset:64\n"
"ds_read_b64_tr_b16 v[70:71], v110 offset:576\n"
"ds_read_b64 a[12:13], v120 offset:3584\n"
"ds_read_b64 a[14:15], v120 offset:3592\n"
"ds_read_b64 a[28:29], v120 offset:3072\n"
"ds_read_b64 a[30:31], v120 offset:3080\n"
"ds_read_b64 a[44:45], v120 offset:2560\n"
"ds_read_b64 a[46:47], v120 offset:2568\n"
"ds_read_b64 a[60:61], v120 offset:2048\n"
"ds_read_b64 a[62:63], v120 offset:2056\n"
"ds_read_b64 a[76:77], v120 offset:1536\n"
"ds_read_b64 a[78:79], v120 offset:1544\n"
"ds_read_b64 a[92:93], v120 offset:1024\n"
"ds_read_b64 a[94:95], v120 offset:1032\n"
"ds_read_b64 a[108:109], v120 offset:512\n"
"ds_read_b64 a[110:111], v120 offset:520\n"
"ds_read_b64 a[124:125], v120 offset:0\n"
"ds_read_b64 a[126:127], v120 offset:8\n"
"ds_read_b64 a[8:9], v120 offset:3712\n"
"ds_read_b64 a[10:11], v120 offset:3720\n"
"ds_read_b64 a[24:25], v120 offset:3200\n"
"ds_read_b64 a[26:27], v120 offset:3208\n"
"ds_read_b64 a[40:41], v120 offset:2688\n"
"ds_read_b64 a[42:43], v120 offset:2696\n"
"ds_read_b64 a[56:57], v120 offset:2176\n"
"ds_read_b64 a[58:59], v120 offset:2184\n"
"buffer_load_dwordx2 v[44:45], v47, s[4:7], s83 offen sc1\n"
"s_waitcnt lgkmcnt(15)\n"
"buffer_store_dwordx4 v[64:67], v60, s[4:7], s18 offen sc1\n"
"buffer_store_dwordx4 v[68:71], v60, s[4:7], s19 offen sc1\n"
"s_waitcnt vmcnt(3)\n"
"v_lshlrev_b32_e64 v160, 16, v4\n"
"v_and_b32 v161, 0xffff0000, v4\n"
"v_pk_fma_f32 v[176:177], v[160:161], v[20:21], v[20:21] op_sel:[0,0,1] op_sel_hi:[1,0,1]\n"
"v_lshlrev_b32_e64 v162, 16, v5\n"
"v_and_b32 v163, 0xffff0000, v5\n"
"v_pk_fma_f32 v[178:179], v[162:163], v[22:23], v[22:23] op_sel:[0,0,1] op_sel_hi:[1,0,1]\n"
"v_lshlrev_b32 v164, 16, v6\n"
"v_and_b32 v165, 0xffff0000, v6\n"
"v_pk_fma_f32 v[180:181], v[164:165], v[24:25], v[24:25] op_sel:[0,0,1] op_sel_hi:[1,0,1]\n"
"v_lshlrev_b32 v166, 16, v7\n"
"v_and_b32 v167, 0xffff0000, v7\n"
"v_pk_fma_f32 v[182:183], v[166:167], v[26:27], v[26:27] op_sel:[0,0,1] op_sel_hi:[1,0,1]\n"
"v_cvt_pk_bf16_f32 v52, v176, v178\n"
"v_cvt_pk_bf16_f32 v53, v180, v182\n"
"ds_write_b64 v121, v[52:53] offset:0\n"
"v_cvt_pk_bf16_f32 v56, v177, v179\n"
"v_cvt_pk_bf16_f32 v57, v181, v183\n"
"ds_write_b64 v121, v[56:57] offset:4576\n"
"v_lshlrev_b32 v168, 16, v8\n"
"v_and_b32 v169, 0xffff0000, v8\n"
"v_pk_fma_f32 v[184:185], v[168:169], v[28:29], v[28:29] op_sel:[0,0,1] op_sel_hi:[1,0,1]\n"
"v_lshlrev_b32 v170, 16, v9\n"
"v_and_b32 v171, 0xffff0000, v9\n"
"v_pk_fma_f32 v[186:187], v[170:171], v[30:31], v[30:31] op_sel:[0,0,1] op_sel_hi:[1,0,1]\n"
"v_lshlrev_b32 v172, 16, v10\n"
"v_and_b32 v173, 0xffff0000, v10\n"
"v_pk_fma_f32 v[188:189], v[172:173], v[32:33], v[32:33] op_sel:[0,0,1] op_sel_hi:[1,0,1]\n"
"v_lshlrev_b32 v174, 16, v11\n"
"v_and_b32 v175, 0xffff0000, v11\n"
"v_pk_fma_f32 v[190:191], v[174:175], v[34:35], v[34:35] op_sel:[0,0,1] op_sel_hi:[1,0,1]\n"
"v_cvt_pk_bf16_f32 v52, v184, v186\n"
"v_cvt_pk_bf16_f32 v53, v188, v190\n"
"ds_write_b64 v121, v[52:53] offset:512\n"
"v_cvt_pk_bf16_f32 v56, v185, v187\n"
"v_cvt_pk_bf16_f32 v57, v189, v191\n"
"ds_write_b64 v121, v[56:57] offset:5088\n"
"s_waitcnt lgkmcnt(0)\n"
"s_barrier\n"
"ds_read_b128 v[92:95], v122 offset:0\n"
"ds_read_b128 v[88:91], v122 offset:128\n"
"ds_read_b128 v[84:87], v122 offset:256\n"
"ds_read_b128 v[80:83], v122 offset:384\n"
"ds_read_b128 v[76:79], v122 offset:512\n"
"s_lshl_b32 s34, s65, 16\n"
"s_and_b32 s35, s65, 0xffff0000\n"
"v_mov_b32 v52, s34\n"
"v_mov_b32 v53, s35\n"
"s_waitcnt lgkmcnt(4)\n"
"v_mfma_f32_16x16x32_bf16 v[128:131], a[12:15], v[92:95], 0\n"
"ds_read_b64 a[72:73], v120 offset:1664\n"
"ds_read_b64 a[74:75], v120 offset:1672\n"
"v_mfma_f32_16x16x32_bf16 v[132:135], a[28:31], v[92:95], 0\n"
"ds_read_b64 a[88:89], v120 offset:1152\n"
"ds_read_b64 a[90:91], v120 offset:1160\n"
"v_mfma_f32_16x16x32_bf16 v[136:139], a[44:47], v[92:95], 0\n"
"ds_read_b64 a[104:105], v120 offset:640\n"
"ds_read_b64 a[106:107], v120 offset:648\n"
"v_mfma_f32_16x16x32_bf16 v[140:143], a[60:63], v[92:95], 0\n"
"ds_read_b64 a[120:121], v120 offset:128\n"
"ds_read_b64 a[122:123], v120 offset:136\n"
"v_mfma_f32_16x16x32_bf16 v[144:147], a[76:79], v[92:95], 0\n"
"ds_read_b64 a[4:5], v120 offset:3840\n"
"ds_read_b64 a[6:7], v120 offset:3848\n"
"v_mfma_f32_16x16x32_bf16 v[148:151], a[92:95], v[92:95], 0\n"
"s_waitcnt vmcnt(2)\n"
"v_cmp_ne_u64_e64 vcc, s[62:63], v[44:45]\n"
"s_cbranch_vccnz L_fw\n"
"L_fw_back:\n"
"s_add_u32 m0, s70, 0\n"
"s_add_u32 s35, s20, 0\n"
"buffer_load_dwordx4 v2, s[4:7], s35 offen sc1 lds\n"
"v_mfma_f32_16x16x32_bf16 v[152:155], a[108:111], v[92:95], 0\n"
"ds_read_b64 a[20:21], v120 offset:3328\n"
"ds_read_b64 a[22:23], v120 offset:3336\n"
"v_mfma_f32_16x16x32_bf16 v[156:159], a[124:127], v[92:95], 0\n"
"ds_read_b64 a[36:37], v120 offset:2816\n"
"ds_read_b64 a[38:39], v120 offset:2824\n"
"s_waitcnt lgkmcnt(15)\n"
"s_nop 0\n"
"v_mfma_f32_16x16x32_bf16 v[128:131], a[8:11], v[88:91], v[128:131]\n"
"ds_read_b64 a[52:53], v120 offset:2304\n"
"ds_read_b64 a[54:55], v120 offset:2312\n"
"v_mfma_f32_16x16x32_bf16 v[132:135], a[24:27], v[88:91], v[132:135]\n"
"ds_read_b64 a[68:69], v120 offset:1792\n"
"ds_read_b64 a[70:71], v120 offset:1800\n"
"v_mfma_f32_16x16x32_bf16 v[136:139], a[40:43], v[88:91], v[136:139]\n"
"ds_read_b64 a[84:85], v120 offset:1280\n"
"ds_read_b64 a[86:87], v120 offset:1288\n"
"buffer_load_dwordx4 v2, s[4:7], s35 offen offset:1024 sc1 lds\n"
"v_mfma_f32_16x16x32_bf16 v[140:143], a[56:59], v[88:91], v[140:143]\n"
"ds_read_b64 a[100:101], v120 offset:768\n"
"ds_read_b64 a[102:103], v120 offset:776\n"
"s_waitcnt lgkmcnt(15)\n"
"v_mfma_f32_16x16x32_bf16 v[144:147], a[72:75], v[88:91], v[144:147]\n"
"ds_read_b64 a[116:117], v120 offset:256\n"
"s_nop 0\n"
"ds_read_b64 a[118:119], v120 offset:264\n"
"v_mfma_f32_16x16x32_bf16 v[148:151], a[88:91], v[88:91], v[148:151]\n"
"ds_read_b64 a[0:1], v120 offset:3968\n"
"ds_read_b64 a[2:3], v120 offset:3976\n"
"v_mfma_f32_16x16x32_bf16 v[152:155], a[104:107], v[88:91], v[152:155]\n"
"ds_read_b64 a[16:17], v120 offset:3456\n"
"ds_read_b64 a[18:19], v120 offset:3464\n"
"s_waitcnt lgkmcnt(15)\n"
"s_nop 0\n"
"v_mfma_f32_16x16x32_bf16 v[156:159], a[120:123], v[88:91], v[156:159]\n"
"ds_read_b64 a[32:33], v120 offset:2944\n"
"ds_read_b64 a[34:35], v120 offset:2952\n"
"buffer_load_dwordx4 v2, s[4:7], s35 offen offset:2048 sc1 lds\n"
"v_mfma_f32_16x16x32_bf16 v[128:131], a[4:7], v[84:87], v[128:131]\n"
"ds_read_b64 a[48:49], v120 offset:2432\n"
"ds_read_b64 a[50:51], v120 offset:2440\n"
"v_mfma_f32_16x16x32_bf16 v[132:135], a[20:23], v[84:87], v[132:135]\n"
"ds_read_b64 a[64:65], v120 offset:1920\n"
"ds_read_b64 a[66:67], v120 offset:1928\n"
"s_waitcnt lgkmcnt(15)\n"
"v_mfma_f32_16x16x32_bf16 v[136:139], a[36:39], v[84:87], v[136:139]\n"
"s_nop 0\n"
"ds_read_b64 a[80:81], v120 offset:1408\n"
"ds_read_b64 a[82:83], v120 offset:1416\n"
"v_mfma_f32_16x16x32_bf16 v[140:143], a[52:55], v[84:87], v[140:143]\n"
"ds_read_b64 a[96:97], v120 offset:896\n"
"ds_read_b64 a[98:99], v120 offset:904\n"
"v_mfma_f32_16x16x32_bf16 v[144:147], a[68:71], v[84:87], v[144:147]\n"
"ds_read_b64 a[112:113], v120 offset:384\n"
"ds_read_b64 a[114:115], v120 offset:392\n"
"buffer_load_dwordx4 v2, s[4:7], s35 offen offset:3072 sc1 lds\n"
"s_waitcnt lgkmcnt(15)\n"
"v_mfma_f32_16x16x32_bf16 v[148:151], a[84:87], v[84:87], v[148:151]\n"
"ds_read_b128 v[72:75], v122 offset:640\n"
"v_mfma_f32_16x16x32_bf16 v[152:155], a[100:103], v[84:87], v[152:155]\n"
"ds_read_b128 v[68:71], v122 offset:768\n"
"v_mfma_f32_16x16x32_bf16 v[156:159], a[116:119], v[84:87], v[156:159]\n"
"ds_read_b128 v[64:67], v122 offset:896\n"
"s_waitcnt lgkmcnt(13)\n"
"v_mfma_f32_16x16x32_bf16 v[128:131], a[0:3], v[80:83], v[128:131]\n"
"ds_read_b128 v[92:95], v122 offset:1024\n"
"v_mfma_f32_16x16x32_bf16 v[132:135], a[16:19], v[80:83], v[132:135]\n"
"s_add_u32 m0, s70, 4096\n"
"s_add_u32 s35, s21, 0\n"
"buffer_load_dwordx4 v2, s[4:7], s35 offen sc1 lds\n"
"v_mfma_f32_16x16x32_bf16 v[136:139], a[32:35], v[80:83], v[136:139]\n"
"s_waitcnt lgkmcnt(8)\n"
"v_mfma_f32_16x16x32_bf16 v[140:143], a[48:51], v[80:83], v[140:143]\n"
"v_mfma_f32_16x16x32_bf16 v[144:147], a[64:67], v[80:83], v[144:147]\n"
"v_cvt_pk_bf16_f32 v128, v128, v129\n"
"v_cvt_pk_bf16_f32 v129, v130, v131\n"
"v_mfma_f32_16x16x32_bf16 v[148:151], a[80:83], v[80:83], v[148:151]\n"
"ds_write_b64 v123, v[128:129] offset:0\n"
"s_waitcnt lgkmcnt(5)\n"
"v_mfma_f32_16x16x32_bf16 v[152:155], a[96:99], v[80:83], v[152:155]\n"
"buffer_load_dwordx4 v2, s[4:7], s35 offen offset:1024 sc1 lds\n"
"s_nop 0\n"
"v_mfma_f32_16x16x32_bf16 v[156:159], a[112:115], v[80:83], v[156:159]\n"
"v_mfma_f32_16x16x32_bf16 v[132:135], a[12:15], v[76:79], v[132:135]\n"
"ds_read_b128 v[88:91], v122 offset:1152\n"
"v_mfma_f32_16x16x32_bf16 v[136:139], a[28:31], v[76:79], v[136:139]\n"
"v_mfma_f32_16x16x32_bf16 v[140:143], a[44:47], v[76:79], v[140:143]\n"
"v_mfma_f32_16x16x32_bf16 v[144:147], a[60:63], v[76:79], v[144:147]\n"
"buffer_load_dwordx4 v2, s[4:7], s35 offen offset:2048 sc1 lds\n"
"v_mfma_f32_16x16x32_bf16 v[148:151], a[76:79], v[76:79], v[148:151]\n"
"v_mfma_f32_16x16x32_bf16 v[152:155], a[92:95], v[76:79], v[152:155]\n"
"v_mfma_f32_16x16x32_bf16 v[156:159], a[108:111], v[76:79], v[156:159]\n"
"s_waitcnt lgkmcnt(5)\n"
"v_mfma_f32_16x16x32_bf16 v[132:135], a[8:11], v[72:75], v[132:135]\n"
"s_nop 0\n"
"ds_read_b128 v[84:87], v122 offset:1280\n"
"v_mfma_f32_16x16x32_bf16 v[136:139], a[24:27], v[72:75], v[136:139]\n"
"buffer_load_dwordx4 v2, s[4:7], s35 offen offset:3072 sc1 lds\n"
"v_mfma_f32_16x16x32_bf16 v[140:143], a[40:43], v[72:75], v[140:143]\n"
"v_mfma_f32_16x16x32_bf16 v[144:147], a[56:59], v[72:75], v[144:147]\n"
"v_mfma_f32_16x16x32_bf16 v[148:151], a[72:75], v[72:75], v[148:151]\n"
"v_mfma_f32_16x16x32_bf16 v[152:155], a[88:91], v[72:75], v[152:155]\n"
"v_mfma_f32_16x16x32_bf16 v[156:159], a[104:107], v[72:75], v[156:159]\n"
"buffer_load_dwordx4 v[192:195], v2, s[4:7], s38 offen offset:0 sc1\n"
"s_waitcnt lgkmcnt(5)\n"
"v_mfma_f32_16x16x32_bf16 v[132:135], a[4:7], v[68:71], v[132:135]\n"
"ds_read_b128 v[80:83], v122 offset:1408\n"
"s_nop 0\n"
"v_mfma_f32_16x16x32_bf16 v[136:139], a[20:23], v[68:71], v[136:139]\n"
"v_mfma_f32_16x16x32_bf16 v[140:143], a[36:39], v[68:71], v[140:143]\n"
"v_mfma_f32_16x16x32_bf16 v[144:147], a[52:55], v[68:71], v[144:147]\n"
"v_mfma_f32_16x16x32_bf16 v[148:151], a[68:71], v[68:71], v[148:151]\n"
"buffer_load_dwordx4 v[196:199], v2, s[4:7], s38 offen offset:1024 sc1\n"
"v_mfma_f32_16x16x32_bf16 v[152:155], a[84:87], v[68:71], v[152:155]\n"
"v_mfma_f32_16x16x32_bf16 v[156:159], a[100:103], v[68:71], v[156:159]\n"
"s_waitcnt lgkmcnt(5)\n"
"s_nop 0\n"
"v_mfma_f32_16x16x32_bf16 v[132:135], a[0:3], v[64:67], v[132:135]\n"
"ds_read_b128 v[76:79], v122 offset:1536\n"
"v_mfma_f32_16x16x32_bf16 v[136:139], a[16:19], v[64:67], v[136:139]\n"
"v_mfma_f32_16x16x32_bf16 v[140:143], a[32:35], v[64:67], v[140:143]\n"
"buffer_load_dwordx4 v[200:203], v2, s[4:7], s38 offen offset:2048 sc1\n"
"v_mfma_f32_16x16x32_bf16 v[144:147], a[48:51], v[64:67], v[144:147]\n"
"v_mfma_f32_16x16x32_bf16 v[148:151], a[64:67], v[64:67], v[148:151]\n"
"s_nop 1\n"
"s_nop 0\n"
"v_cvt_pk_bf16_f32 v132, v132, v133\n"
"v_cvt_pk_bf16_f32 v133, v134, v135\n"
"v_mfma_f32_16x16x32_bf16 v[152:155], a[80:83], v[64:67], v[152:155]\n"
"ds_write_b64 v123, v[132:133] offset:512\n"
"v_mfma_f32_16x16x32_bf16 v[156:159], a[96:99], v[64:67], v[156:159]\n"
"s_waitcnt lgkmcnt(6)\n"
"v_mfma_f32_16x16x32_bf16 v[136:139], a[12:15], v[92:95], v[136:139]\n"
"ds_read_b128 v[72:75], v122 offset:1664\n"
"s_nop 0\n"
"buffer_load_dwordx4 v[204:207], v2, s[4:7], s38 offen offset:3072 sc1\n"
"v_mfma_f32_16x16x32_bf16 v[140:143], a[28:31], v[92:95], v[140:143]\n"
"v_mfma_f32_16x16x32_bf16 v[144:147], a[44:47], v[92:95], v[144:147]\n"
"v_mfma_f32_16x16x32_bf16 v[148:151], a[60:63], v[92:95], v[148:151]\n"
"v_mfma_f32_16x16x32_bf16 v[152:155], a[76:79], v[92:95], v[152:155]\n"
"v_mfma_f32_16x16x32_bf16 v[156:159], a[92:95], v[92:95], v[156:159]\n"
"buffer_load_dwordx4 v[208:211], v2, s[4:7], s39 offen offset:0 sc1\n"
"s_waitcnt lgkmcnt(5)\n"
"s_nop 0\n"
"v_mfma_f32_16x16x32_bf16 v[136:139], a[8:11], v[88:91], v[136:139]\n"
"ds_read_b128 v[68:71], v122 offset:1792\n"
"v_mfma_f32_16x16x32_bf16 v[140:143], a[24:27], v[88:91], v[140:143]\n"
"v_mfma_f32_16x16x32_bf16 v[144:147], a[40:43], v[88:91], v[144:147]\n"
"v_mfma_f32_16x16x32_bf16 v[148:151], a[56:59], v[88:91], v[148:151]\n"
"v_mfma_f32_16x16x32_bf16 v[152:155], a[72:75], v[88:91], v[152:155]\n"
"buffer_load_dwordx4 v[212:215], v2, s[4:7], s39 offen offset:1024 sc1\n"
"v_mfma_f32_16x16x32_bf16 v[156:159], a[88:91], v[88:91], v[156:159]\n"
"s_waitcnt lgkmcnt(5)\n"
"v_mfma_f32_16x16x32_bf16 v[136:139], a[4:7], v[84:87], v[136:139]\n"
"ds_read_b128 v[64:67], v122 offset:1920\n"
"v_mfma_f32_16x16x32_bf16 v[140:143], a[20:23], v[84:87], v[140:143]\n"
"s_nop 0\n"
"v_mfma_f32_16x16x32_bf16 v[144:147], a[36:39], v[84:87], v[144:147]\n"
"v_mfma_f32_16x16x32_bf16 v[148:151], a[52:55], v[84:87], v[148:151]\n"
"buffer_load_dwordx4 v[216:219], v2, s[4:7], s39 offen offset:2048 sc1\n"
"v_mfma_f32_16x16x32_bf16 v[152:155], a[68:71], v[84:87], v[152:155]\n"
"v_mfma_f32_16x16x32_bf16 v[156:159], a[84:87], v[84:87], v[156:159]\n"
"s_waitcnt lgkmcnt(5)\n"
"v_mfma_f32_16x16x32_bf16 v[136:139], a[0:3], v[80:83], v[136:139]\n"
"ds_read_b128 v[92:95], v122 offset:2048\n"
"s_nop 0\n"
"v_mfma_f32_16x16x32_bf16 v[140:143], a[16:19], v[80:83], v[140:143]\n"
"v_mfma_f32_16x16x32_bf16 v[144:147], a[32:35], v[80:83], v[144:147]\n"
"s_add_u32 m0, s70, 32768\n"
"s_add_u32 s35, s20, 32768\n"
"buffer_load_dwordx4 v2, s[4:7], s35 offen sc1 lds\n"
"v_mfma_f32_16x16x32_bf16 v[148:151], a[48:51], v[80:83], v[148:151]\n"
"v_mfma_f32_16x16x32_bf16 v[152:155], a[64:67], v[80:83], v[152:155]\n"
"v_cvt_pk_bf16_f32 v136, v136, v137\n"
"v_cvt_pk_bf16_f32 v137, v138, v139\n"
"v_mfma_f32_16x16x32_bf16 v[156:159], a[80:83], v[80:83], v[156:159]\n"
"ds_write_b64 v123, v[136:137] offset:1024\n"
"s_waitcnt lgkmcnt(6)\n"
"s_nop 0\n"
"v_mfma_f32_16x16x32_bf16 v[140:143], a[12:15], v[76:79], v[140:143]\n"
"ds_read_b128 v[88:91], v122 offset:2176\n"
"v_mfma_f32_16x16x32_bf16 v[144:147], a[28:31], v[76:79], v[144:147]\n"
"buffer_load_dwordx4 v2, s[4:7], s35 offen offset:1024 sc1 lds\n"
"v_mfma_f32_16x16x32_bf16 v[148:151], a[44:47], v[76:79], v[148:151]\n"
"v_mfma_f32_16x16x32_bf16 v[152:155], a[60:63], v[76:79], v[152:155]\n"
"v_mfma_f32_16x16x32_bf16 v[156:159], a[76:79], v[76:79], v[156:159]\n"
"s_waitcnt lgkmcnt(5)\n"
"s_nop 0\n"
"v_mfma_f32_16x16x32_bf16 v[140:143], a[8:11], v[72:75], v[140:143]\n"
"ds_read_b128 v[84:87], v122 offset:2304\n"
"v_mfma_f32_16x16x32_bf16 v[144:147], a[24:27], v[72:75], v[144:147]\n"
"buffer_load_dwordx4 v2, s[4:7], s35 offen offset:2048 sc1 lds\n"
"v_mfma_f32_16x16x32_bf16 v[148:151], a[40:43], v[72:75], v[148:151]\n"
"v_mfma_f32_16x16x32_bf16 v[152:155], a[56:59], v[72:75], v[152:155]\n"
"v_mfma_f32_16x16x32_bf16 v[156:159], a[72:75], v[72:75], v[156:159]\n"
"s_waitcnt lgkmcnt(5)\n"
"s_nop 0\n"
"v_mfma_f32_16x16x32_bf16 v[140:143], a[4:7], v[68:71], v[140:143]\n"
"ds_read_b128 v[80:83], v122 offset:2432\n"
"v_mfma_f32_16x16x32_bf16 v[144:147], a[20:23], v[68:71], v[144:147]\n"
"buffer_load_dwordx4 v2, s[4:7], s35 offen offset:3072 sc1 lds\n"
"v_mfma_f32_16x16x32_bf16 v[148:151], a[36:39], v[68:71], v[148:151]\n"
"v_mfma_f32_16x16x32_bf16 v[152:155], a[52:55], v[68:71], v[152:155]\n"
"v_mfma_f32_16x16x32_bf16 v[156:159], a[68:71], v[68:71], v[156:159]\n"
"s_waitcnt lgkmcnt(5)\n"
"s_nop 0\n"
"v_mfma_f32_16x16x32_bf16 v[140:143], a[0:3], v[64:67], v[140:143]\n"
"ds_read_b128 v[76:79], v122 offset:2560\n"
"v_mfma_f32_16x16x32_bf16 v[144:147], a[16:19], v[64:67], v[144:147]\n"
"s_add_u32 m0, s70, 36864\n"
"s_add_u32 s35, s21, 32768\n"
"buffer_load_dwordx4 v2, s[4:7], s35 offen sc1 lds\n"
"v_mfma_f32_16x16x32_bf16 v[148:151], a[32:35], v[64:67], v[148:151]\n"
"v_mfma_f32_16x16x32_bf16 v[152:155], a[48:51], v[64:67], v[152:155]\n"
"v_mfma_f32_16x16x32_bf16 v[156:159], a[64:67], v[64:67], v[156:159]\n"
"v_cvt_pk_bf16_f32 v140, v140, v141\n"
"v_cvt_pk_bf16_f32 v141, v142, v143\n"
"s_waitcnt lgkmcnt(5)\n"
"s_nop 0\n"
"v_mfma_f32_16x16x32_bf16 v[144:147], a[12:15], v[92:95], v[144:147]\n"
"ds_read_b128 v[72:75], v122 offset:2688\n"
"ds_write_b64 v123, v[140:141] offset:1536\n"
"v_mfma_f32_16x16x32_bf16 v[148:151], a[28:31], v[92:95], v[148:151]\n"
"buffer_load_dwordx4 v2, s[4:7], s35 offen offset:1024 sc1 lds\n"
"v_mfma_f32_16x16x32_bf16 v[152:155], a[44:47], v[92:95], v[152:155]\n"
"v_mfma_f32_16x16x32_bf16 v[156:159], a[60:63], v[92:95], v[156:159]\n"
"s_waitcnt lgkmcnt(5)\n"
"s_nop 0\n"
"v_mfma_f32_16x16x32_bf16 v[144:147], a[8:11], v[88:91], v[144:147]\n"
"ds_read_b128 v[68:71], v122 offset:2816\n"
"v_mfma_f32_16x16x32_bf16 v[148:151], a[24:27], v[88:91], v[148:151]\n"
"v_mfma_f32_16x16x32_bf16 v[152:155], a[40:43], v[88:91], v[152:155]\n"
"buffer_load_dwordx4 v2, s[4:7], s35 offen offset:2048 sc1 lds\n"
"v_mfma_f32_16x16x32_bf16 v[156:159], a[56:59], v[88:91], v[156:159]\n"
"s_waitcnt lgkmcnt(5)\n"
"v_mfma_f32_16x16x32_bf16 v[144:147], a[4:7], v[84:87], v[144:147]\n"
"s_nop 0\n"
"ds_read_b128 v[64:67], v122 offset:2944\n"
"v_mfma_f32_16x16x32_bf16 v[148:151], a[20:23], v[84:87], v[148:151]\n"
"v_mfma_f32_16x16x32_bf16 v[152:155], a[36:39], v[84:87], v[152:155]\n"
"v_mfma_f32_16x16x32_bf16 v[156:159], a[52:55], v[84:87], v[156:159]\n"
"buffer_load_dwordx4 v2, s[4:7], s35 offen offset:3072 sc1 lds\n"
"s_waitcnt lgkmcnt(5)\n"
"v_mfma_f32_16x16x32_bf16 v[144:147], a[0:3], v[80:83], v[144:147]\n"
"ds_read_b128 v[92:95], v122 offset:3072\n"
"s_nop 0\n"
"v_mfma_f32_16x16x32_bf16 v[148:151], a[16:19], v[80:83], v[148:151]\n"
"ds_read_b128 v[88:91], v122 offset:3200\n"
"v_mfma_f32_16x16x32_bf16 v[152:155], a[32:35], v[80:83], v[152:155]\n"
"v_mfma_f32_16x16x32_bf16 v[156:159], a[48:51], v[80:83], v[156:159]\n"
"ds_read_b128 v[84:87], v122 offset:3328\n"
"s_waitcnt lgkmcnt(7)\n"
"v_mfma_f32_16x16x32_bf16 v[148:151], a[12:15], v[76:79], v[148:151]\n"
"s_add_u32 m0, s70, 65536\n"
"s_nop 0\n"
"s_add_u32 s35, s20, 65536\n"
"buffer_load_dwordx4 v2, s[4:7], s35 offen sc1 lds\n"
"v_cvt_pk_bf16_f32 v144, v144, v145\n"
"v_mfma_f32_16x16x32_bf16 v[152:155], a[28:31], v[76:79], v[152:155]\n"
"v_cvt_pk_bf16_f32 v145, v146, v147\n"
"ds_write_b64 v123, v[144:145] offset:2048\n"
"v_mfma_f32_16x16x32_bf16 v[156:159], a[44:47], v[76:79], v[156:159]\n"
"ds_read_b128 v[80:83], v122 offset:3456\n"
"s_waitcnt lgkmcnt(8)\n"
"v_mfma_f32_16x16x32_bf16 v[148:151], a[8:11], v[72:75], v[148:151]\n"
"v_mfma_f32_16x16x32_bf16 v[152:155], a[24:27], v[72:75], v[152:155]\n"
"v_mfma_f32_16x16x32_bf16 v[156:159], a[40:43], v[72:75], v[156:159]\n"
"ds_read_b128 v[76:79], v122 offset:3584\n"
"buffer_load_dwordx4 v2, s[4:7], s35 offen offset:1024 sc1 lds\n"
"s_waitcnt lgkmcnt(7)\n"
"v_mfma_f32_16x16x32_bf16 v[148:151], a[4:7], v[68:71], v[148:151]\n"
"v_mfma_f32_16x16x32_bf16 v[152:155], a[20:23], v[68:71], v[152:155]\n"
"v_mfma_f32_16x16x32_bf16 v[156:159], a[36:39], v[68:71], v[156:159]\n"
"ds_read_b128 v[72:75], v122 offset:3712\n"
"s_waitcnt lgkmcnt(7)\n"
"v_mfma_f32_16x16x32_bf16 v[148:151], a[0:3], v[64:67], v[148:151]\n"
"v_mfma_f32_16x16x32_bf16 v[152:155], a[16:19], v[64:67], v[152:155]\n"
"buffer_load_dwordx4 v2, s[4:7], s35 offen offset:2048 sc1 lds\n"
"v_mfma_f32_16x16x32_bf16 v[156:159], a[32:35], v[64:67], v[156:159]\n"
"ds_read_b128 v[68:71], v122 offset:3840\n"
"s_waitcnt lgkmcnt(6)\n"
"v_mfma_f32_16x16x32_bf16 v[152:155], a[12:15], v[92:95], v[152:155]\n"
"v_mfma_f32_16x16x32_bf16 v[156:159], a[28:31], v[92:95], v[156:159]\n"
"ds_read_b128 v[64:67], v122 offset:3968\n"
"v_cvt_pk_bf16_f32 v148, v148, v149\n"
"v_mfma_f32_16x16x32_bf16 v[152:155], a[8:11], v[88:91], v[152:155]\n"
"v_cvt_pk_bf16_f32 v149, v150, v151\n"
"ds_write_b64 v123, v[148:149] offset:2560\n"
"v_mfma_f32_16x16x32_bf16 v[156:159], a[24:27], v[88:91], v[156:159]\n"
"buffer_load_dwordx4 v2, s[4:7], s35 offen offset:3072 sc1 lds\n"
"s_waitcnt lgkmcnt(5)\n"
"v_mfma_f32_16x16x32_bf16 v[152:155], a[4:7], v[84:87], v[152:155]\n"
"v_mfma_f32_16x16x32_bf16 v[156:159], a[20:23], v[84:87], v[156:159]\n"
"v_mfma_f32_16x16x32_bf16 v[152:155], a[0:3], v[80:83], v[152:155]\n"
"v_mfma_f32_16x16x32_bf16 v[156:159], a[16:19], v[80:83], v[156:159]\n"
"s_waitcnt lgkmcnt(3)\n"
"v_mfma_f32_16x16x32_bf16 v[156:159], a[12:15], v[76:79], v[156:159]\n"
"v_mfma_f32_16x16x32_bf16 v[156:159], a[8:11], v[72:75], v[156:159]\n"
"s_waitcnt lgkmcnt(2)\n"
"v_mfma_f32_16x16x32_bf16 v[156:159], a[4:7], v[68:71], v[156:159]\n"
"s_nop 1\n"
"v_cvt_pk_bf16_f32 v152, v152, v153\n"
"v_cvt_pk_bf16_f32 v153, v154, v155\n"
"s_waitcnt lgkmcnt(1)\n"
"v_mfma_f32_16x16x32_bf16 v[156:159], a[0:3], v[64:67], v[156:159]\n"
"ds_write_b64 v123, v[152:153] offset:3072\n"
"v_pk_fma_f32 v[20:21], v[176:177], v[52:53], v[160:161]\n"
"s_nop 0\n"
"v_pk_fma_f32 v[22:23], v[178:179], v[52:53], v[162:163]\n"
"v_pk_fma_f32 v[24:25], v[180:181], v[52:53], v[164:165]\n"
"v_pk_fma_f32 v[26:27], v[182:183], v[52:53], v[166:167]\n"
"v_pk_fma_f32 v[28:29], v[184:185], v[52:53], v[168:169]\n"
"v_pk_fma_f32 v[30:31], v[186:187], v[52:53], v[170:171]\n"
"v_pk_fma_f32 v[32:33], v[188:189], v[52:53], v[172:173]\n"
"v_pk_fma_f32 v[34:35], v[190:191], v[52:53], v[174:175]\n"
"v_cvt_pk_bf16_f32 v156, v156, v157\n"
"v_cvt_pk_bf16_f32 v157, v158, v159\n"
"ds_write_b64 v123, v[156:157] offset:3584\n"
"s_waitcnt lgkmcnt(0)\n"
"s_barrier\n"
"ds_read_b64 v[64:65], v121 offset:8672\n"
"ds_read_b64 v[66:67], v121 offset:12768\n"
"ds_read_b64 v[68:69], v121 offset:16864\n"
"ds_read_b64 v[70:71], v121 offset:20960\n"
"ds_read_b64 v[72:73], v121 offset:9184\n"
"ds_read_b64 v[74:75], v121 offset:13280\n"
"ds_read_b64 v[76:77], v121 offset:17376\n"
"ds_read_b64 v[78:79], v121 offset:21472\n"
"s_waitcnt lgkmcnt(4)\n"
"v_lshlrev_b32 v80, 16, v64\n"
"v_lshlrev_b32 v81, 16, v68\n"
"v_lshlrev_b32 v82, 16, v66\n"
"v_lshlrev_b32_e64 v83, 16, v70\n"
"v_pk_add_f32 v[80:81], v[80:81], v[20:21]\n"
"v_pk_add_f32 v[80:81], v[80:81], v[82:83]\n"
"v_cvt_pk_bf16_f32 v88, v80, v81\n"
"v_and_b32 v84, 0xffff0000, v64\n"
"v_and_b32 v85, 0xffff0000, v68\n"
"v_and_b32 v86, 0xffff0000, v66\n"
"v_and_b32 v87, 0xffff0000, v70\n"
"v_pk_add_f32 v[84:85], v[84:85], v[22:23]\n"
"v_pk_add_f32 v[84:85], v[84:85], v[86:87]\n"
"v_cvt_pk_bf16_f32 v89, v84, v85\n"
"v_lshlrev_b32 v80, 16, v65\n"
"v_lshlrev_b32 v81, 16, v69\n"
"v_lshlrev_b32 v82, 16, v67\n"
"v_lshlrev_b32 v83, 16, v71\n"
"v_pk_add_f32 v[80:81], v[80:81], v[24:25]\n"
"v_pk_add_f32 v[80:81], v[80:81], v[82:83]\n"
"v_cvt_pk_bf16_f32 v90, v80, v81\n"
"v_and_b32 v84, 0xffff0000, v65\n"
"v_and_b32 v85, 0xffff0000, v69\n"
"v_and_b32 v86, 0xffff0000, v67\n"
"v_and_b32 v87, 0xffff0000, v71\n"
"v_pk_add_f32 v[84:85], v[84:85], v[26:27]\n"
"v_pk_add_f32 v[84:85], v[84:85], v[86:87]\n"
"v_cvt_pk_bf16_f32 v91, v84, v85\n"
"buffer_store_dwordx4 v[88:91], v107, s[4:7], s84 offen sc1\n"
"s_waitcnt lgkmcnt(0)\n"
"v_lshlrev_b32 v80, 16, v72\n"
"v_lshlrev_b32 v81, 16, v76\n"
"v_lshlrev_b32 v82, 16, v74\n"
"v_lshlrev_b32_e64 v83, 16, v78\n"
"v_pk_add_f32 v[80:81], v[80:81], v[28:29]\n"
"v_pk_add_f32 v[80:81], v[80:81], v[82:83]\n"
"v_cvt_pk_bf16_f32 v92, v80, v81\n"
"v_and_b32 v84, 0xffff0000, v72\n"
"v_and_b32 v85, 0xffff0000, v76\n"
"v_and_b32 v86, 0xffff0000, v74\n"
"v_and_b32 v87, 0xffff0000, v78\n"
"v_pk_add_f32 v[84:85], v[84:85], v[30:31]\n"
"v_pk_add_f32 v[84:85], v[84:85], v[86:87]\n"
"v_cvt_pk_bf16_f32 v93, v84, v85\n"
"v_lshlrev_b32 v80, 16, v73\n"
"v_lshlrev_b32 v81, 16, v77\n"
"v_lshlrev_b32 v82, 16, v75\n"
"v_lshlrev_b32 v83, 16, v79\n"
"v_pk_add_f32 v[80:81], v[80:81], v[32:33]\n"
"v_pk_add_f32 v[80:81], v[80:81], v[82:83]\n"
"v_cvt_pk_bf16_f32 v94, v80, v81\n"
"v_and_b32 v84, 0xffff0000, v73\n"
"v_and_b32 v85, 0xffff0000, v77\n"
"v_and_b32 v86, 0xffff0000, v75\n"
"v_and_b32 v87, 0xffff0000, v79\n"
"v_pk_add_f32 v[84:85], v[84:85], v[34:35]\n"
"v_pk_add_f32 v[84:85], v[84:85], v[86:87]\n"
"v_cvt_pk_bf16_f32 v95, v84, v85\n"
"buffer_store_dwordx4 v[92:95], v107, s[4:7], s89 offen sc1\n"
"s_add_u32 m0, s70, 69632\n"
"s_add_u32 s35, s21, 65536\n"
"buffer_load_dwordx4 v2, s[4:7], s35 offen sc1 lds\n"
"buffer_load_dwordx4 v2, s[4:7], s35 offen offset:1024 sc1 lds\n"
"buffer_load_dwordx4 v2, s[4:7], s35 offen offset:2048 sc1 lds\n"
"buffer_load_dwordx4 v2, s[4:7], s35 offen offset:3072 sc1 lds\n"
"s_add_u32 m0, s70, 98304\n"
"s_add_u32 s35, s20, 98304\n"
"buffer_load_dwordx4 v2, s[4:7], s35 offen sc1 lds\n"
"buffer_load_dwordx4 v2, s[4:7], s35 offen offset:1024 sc1 lds\n"
"buffer_load_dwordx4 v2, s[4:7], s35 offen offset:2048 sc1 lds\n"
"buffer_load_dwordx4 v2, s[4:7], s35 offen offset:3072 sc1 lds\n"
"v_lshlrev_b32 v40, 11, v0\n"
"s_lshr_b32 s34, s2, 3\n"
"s_and_b32 s34, s34, 15\n"
"s_lshl_b32 s34, s34, 7\n"
"s_add_u32 s23, s34, 69632\n"
"v_mov_b32 v46, 2130706432\n"
"v_cmp_gt_u32 vcc, 16, v0\n"
"v_mov_b32 v41, 1\n"
"v_lshlrev_b32 v48, 7, v0\n"
"v_cndmask_b32 v40, v46, v40, vcc\n"
"v_cndmask_b32 v49, v46, v48, vcc\n"
"s_and_b32 s34, s2, 15\n"
"s_lshl_b32 s34, s34, 11\n"
"s_add_u32 s83, s34, 69632\n"
"s_cmp_eq_u32 s2, 0\n"
"s_cselect_b32 s34, 0, 2130706432\n"
"v_mov_b32 v52, s60\n"
"v_mov_b32 v53, s61\n"
"v_or_b32 v54, s34, v124\n"
"s_waitcnt vmcnt(10)\n"
"v_permlane32_swap_b32 v192, v193\n"
"v_permlane32_swap_b32 v194, v195\n"
"v_permlane32_swap_b32 v196, v197\n"
"v_permlane32_swap_b32 v198, v199\n"
"v_permlane32_swap_b32 v200, v201\n"
"v_permlane32_swap_b32 v202, v203\n"
"v_permlane32_swap_b32 v204, v205\n"
"v_permlane32_swap_b32 v206, v207\n"
"v_permlane32_swap_b32 v208, v209\n"
"v_permlane32_swap_b32 v210, v211\n"
"v_permlane32_swap_b32 v212, v213\n"
"v_permlane32_swap_b32 v214, v215\n"
"v_permlane32_swap_b32 v216, v217\n"
"v_permlane32_swap_b32 v218, v219\n"
"v_add_f32 v192, v192, v193\n"
"v_add_f32 v194, v194, v195\n"
"v_add_f32 v196, v196, v197\n"
"v_add_f32 v198, v198, v199\n"
"v_add_f32 v200, v200, v201\n"
"v_add_f32 v202, v202, v203\n"
"v_add_f32 v204, v204, v205\n"
"v_add_f32 v206, v206, v207\n"
"v_add_f32 v208, v208, v209\n"
"v_add_f32 v210, v210, v211\n"
"v_add_f32 v212, v212, v213\n"
"v_add_f32 v214, v214, v215\n"
"v_add_f32 v216, v216, v217\n"
"v_add_f32 v218, v218, v219\n"
"v_add_f32_dpp v192, v192, v192 row_ror:8 row_mask:0xf bank_mask:0xf\n"
"v_add_f32_dpp v194, v194, v194 row_ror:8 row_mask:0xf bank_mask:0xf\n"
"v_add_f32_dpp v196, v196, v196 row_ror:8 row_mask:0xf bank_mask:0xf\n"
"v_add_f32_dpp v198, v198, v198 row_ror:8 row_mask:0xf bank_mask:0xf\n"
"v_add_f32_dpp v200, v200, v200 row_ror:8 row_mask:0xf bank_mask:0xf\n"
"v_add_f32_dpp v202, v202, v202 row_ror:8 row_mask:0xf bank_mask:0xf\n"
"v_add_f32_dpp v204, v204, v204 row_ror:8 row_mask:0xf bank_mask:0xf\n"
"v_add_f32_dpp v206, v206, v206 row_ror:8 row_mask:0xf bank_mask:0xf\n"
"v_add_f32_dpp v208, v208, v208 row_ror:8 row_mask:0xf bank_mask:0xf\n"
"v_add_f32_dpp v210, v210, v210 row_ror:8 row_mask:0xf bank_mask:0xf\n"
"v_add_f32_dpp v212, v212, v212 row_ror:8 row_mask:0xf bank_mask:0xf\n"
"v_add_f32_dpp v214, v214, v214 row_ror:8 row_mask:0xf bank_mask:0xf\n"
"v_add_f32_dpp v216, v216, v216 row_ror:8 row_mask:0xf bank_mask:0xf\n"
"v_add_f32_dpp v218, v218, v218 row_ror:8 row_mask:0xf bank_mask:0xf\n"
"v_mov_b32_dpp v192, v194 quad_perm:[0,1,2,3] row_mask:0xf bank_mask:0xc\n"
"v_mov_b32_dpp v196, v198 quad_perm:[0,1,2,3] row_mask:0xf bank_mask:0xc\n"
"v_mov_b32_dpp v200, v202 quad_perm:[0,1,2,3] row_mask:0xf bank_mask:0xc\n"
"v_mov_b32_dpp v204, v206 quad_perm:[0,1,2,3] row_mask:0xf bank_mask:0xc\n"
"v_mov_b32_dpp v208, v210 quad_perm:[0,1,2,3] row_mask:0xf bank_mask:0xc\n"
"v_mov_b32_dpp v212, v214 quad_perm:[0,1,2,3] row_mask:0xf bank_mask:0xc\n"
"v_mov_b32_dpp v216, v218 quad_perm:[0,1,2,3] row_mask:0xf bank_mask:0xc\n"
"v_mov_b32 v193, v192\n"
"v_mov_b32 v197, v196\n"
"v_mov_b32 v201, v200\n"
"v_mov_b32 v205, v204\n"
"v_mov_b32 v209, v208\n"
"v_mov_b32 v213, v212\n"
"v_mov_b32 v217, v216\n"
"v_permlane16_swap_b32 v192, v193\n"
"v_permlane16_swap_b32 v196, v197\n"
"v_permlane16_swap_b32 v200, v201\n"
"v_permlane16_swap_b32 v204, v205\n"
"v_permlane16_swap_b32 v208, v209\n"
"v_permlane16_swap_b32 v212, v213\n"
"v_permlane16_swap_b32 v216, v217\n"
"v_add_f32 v56, v192, v193\n"
"v_add_f32 v57, v196, v197\n"
"v_add_f32 v58, v200, v201\n"
"v_add_f32 v59, v204, v205\n"
"v_add_f32 v60, v208, v209\n"
"v_add_f32 v61, v212, v213\n"
"v_add_f32_e64 v62, v216, v217\n"
"s_waitcnt vmcnt(8)\n"
"s_barrier\n"
"buffer_atomic_add v41, v40, s[4:7], s23 offen sc1\n"
"buffer_store_dwordx2 v[52:53], v54, s[4:7], 0 offen sc1\n"
"s_add_u32 m0, s70, 102400\n"
"s_add_u32 s35, s21, 98304\n"
"buffer_load_dwordx4 v2, s[4:7], s35 offen sc1 lds\n"
"buffer_load_dwordx4 v2, s[4:7], s35 offen offset:1024 sc1 lds\n"
"buffer_load_dwordx4 v2, s[4:7], s35 offen offset:2048 sc1 lds\n"
"buffer_load_dwordx4 v2, s[4:7], s35 offen offset:3072 sc1 lds\n"
"s_add_u32 m0, s70, 131072\n"
"s_add_u32 s35, s20, 131072\n"
"buffer_load_dwordx4 v2, s[4:7], s35 offen sc1 lds\n"
"buffer_load_dwordx4 v2, s[4:7], s35 offen offset:1024 sc1 lds\n"
"buffer_load_dwordx4 v2, s[4:7], s35 offen offset:2048 sc1 lds\n"
"buffer_load_dwordx4 v2, s[4:7], s35 offen offset:3072 sc1 lds\n"
"s_add_u32 m0, s70, 135168\n"
"s_add_u32 s35, s21, 131072\n"
"buffer_load_dwordx4 v2, s[4:7], s35 offen sc1 lds\n"
"buffer_load_dwordx4 v2, s[4:7], s35 offen offset:1024 sc1 lds\n"
"buffer_load_dwordx4 v2, s[4:7], s35 offen offset:2048 sc1 lds\n"
"buffer_load_dwordx4 v2, s[4:7], s35 offen offset:3072 sc1 lds\n"
"s_lshr_b32 s34, s3, 1\n"
"s_lshl_b32 s35, s33, 1\n"
"s_add_u32 s35, s35, s34\n"
"s_mul_i32 s38, s3, 28\n"
"s_lshl_b32 s91, s35, 6\n"
"s_lshl_b32 s34, s35, 11\n"
"s_add_u32 s90, s34, 3145728\n"
"s_mul_i32 s34, s35, 112\n"
"s_add_u32 s34, s34, s38\n"
"s_lshl_b32 s34, s34, 10\n"
"s_add_u32 s34, s34, 9437184\n"
"v_add_u32 v42, s34, v2\n"
"s_xor_b32 s35, s35, 1\n"
"s_mul_i32 s34, s35, 112\n"
"s_add_u32 s34, s34, s38\n"
"s_lshl_b32 s34, s34, 10\n"
"s_add_u32 s34, s34, 9437184\n"
"v_add_u32 v43, s34, v2\n"
"s_and_b32 s34, s3, 1\n"
"s_lshl_b32 s35, s31, 1\n"
"s_add_u32 s35, s35, s34\n"
"s_lshl_b32 s34, s35, 15\n"
"s_add_u32 s90, s90, s34\n"
"s_add_u32 s91, s91, s34\n"
"s_mul_i32 s34, s35, 112\n"
"s_add_u32 s34, s34, s38\n"
"s_lshl_b32 s34, s34, 10\n"
"s_add_u32 s34, s34, 11534336\n"
"v_add_u32 v44, s34, v2\n"
"s_xor_b32 s35, s35, 1\n"
"s_mul_i32 s34, s35, 112\n"
"s_add_u32 s34, s34, s38\n"
"s_lshl_b32 s34, s34, 10\n"
"s_add_u32 s34, s34, 11534336\n"
"v_add_u32 v45, s34, v2\n"
"s_mov_b32 s71, 0\n"
"s_mov_b32 s72, 4096\n"
"s_mov_b32 s73, 7168\n"
"s_mov_b32 s74, 11264\n"
"s_nop 0\n"
"s_mov_b32 s75, 14336\n"
"s_mov_b32 s76, 18432\n"
"s_mov_b32 s77, 21504\n"
"s_mov_b32 s78, 25600\n"
"v_mov_b32 v50, 0x3f803f80\n"
"v_mov_b32 v51, 0\n"
"s_mov_b32 vcc_lo, 0x10100101\n"
"s_mov_b32 vcc_hi, 0x10100101\n"
"v_cndmask_b32 v50, v51, v50, vcc\n"
"v_accvgpr_write_b32 a244, v50\n"
"v_accvgpr_write_b32 a245, v50\n"
"v_accvgpr_write_b32 a246, v50\n"
"v_accvgpr_write_b32 a247, v50\n"
"v_add_u32 v47, 0x10000, v2\n"
"v_add_u32 v50, 0x20000, v2\n"
"v_lshrrev_b32_e64 v53, 5, v1\n"
"v_bfe_u32 v52, v1, 3, 1\n"
"v_bfe_u32 v54, v1, 4, 1\n"
"v_lshl_add_u32 v52, v52, 1, v54\n"
"v_lshl_add_u32 v51, v53, 2, v52\n"
"v_lshlrev_b32 v51, 7, v51\n"
"v_and_b32 v54, 7, v1\n"
"v_lshl_add_u32 v51, v54, 4, v51\n"
"v_lshl_add_u32 v52, v54, 2, v52\n"
"v_lshlrev_b32 v17, 10, v52\n"
"v_lshl_add_u32 v17, v53, 4, v17\n"
"s_lshl_b32 s34, s29, 17\n"
"s_lshl_b32 s35, s3, 15\n"
"s_add_u32 s34, s34, s35\n"
"s_add_u32 s39, s34, 3145728\n"
"v_and_b32 v4, 1, v1\n"
"v_lshlrev_b32 v4, 4, v4\n"
"v_bfe_u32 v5, v1, 1, 2\n"
"v_add_u32 v4, v4, v5\n"
"s_lshl_b32 s34, s31, 5\n"
"s_lshl_b32 s35, s3, 2\n"
"s_add_u32 s34, s34, s35\n"
"v_add_lshl_u32 v4, v4, s34, 9\n"
"v_cmp_gt_u32_e64 vcc, 8, v1\n"
"s_mov_b32 s37, 131072\n"
"v_mov_b32 v36, s62\n"
"v_mov_b32 v37, s63\n"
"v_cndmask_b32 v5, v46, v2, vcc\n"
"v_mov_b32 v38, s62\n"
"v_mov_b32 v39, s63\n"
"s_waitcnt vmcnt(12)\n"
"s_cmp_eq_u32 s3, 0\n"
"s_cbranch_scc0 L_f2done\n"
"s_mov_b32 s66, 65536\n"
"L_f2loop:\n"
"v_mov_b32 v48, 0\n"
"buffer_atomic_add v48, v49, s[4:7], s83 offen sc0 sc1\n"
"s_waitcnt vmcnt(0)\n"
"v_cmp_ne_u32 vcc, 16, v48\n"
"s_nop 0\n"
"s_and_b64 vcc, vcc, 0xffff\n"
"s_cbranch_vccz L_f2done\n"
"s_sub_u32 s66, s66, 1\n"
"s_cbranch_scc0 L_f2loop\n"
"L_f2done:\n"
"s_barrier\n"
"buffer_load_dwordx4 v[128:131], v51, s[4:7], s39 offen offset:0 sc1\n"
"buffer_load_dwordx4 v[132:135], v51, s[4:7], s39 offen offset:1024 sc1\n"
"buffer_load_dwordx4 v[136:139], v51, s[4:7], s39 offen offset:2048 sc1\n"
"buffer_load_dwordx4 v[140:143], v51, s[4:7], s39 offen offset:3072 sc1\n"
"s_add_u32 s39, s39, 4096\n"
"buffer_load_dwordx4 v[144:147], v51, s[4:7], s39 offen offset:0 sc1\n"
"buffer_load_dwordx4 v[148:151], v51, s[4:7], s39 offen offset:1024 sc1\n"
"buffer_load_dwordx4 v[152:155], v51, s[4:7], s39 offen offset:2048 sc1\n"
"buffer_load_dwordx4 v[156:159], v51, s[4:7], s39 offen offset:3072 sc1\n"
"s_add_u32 s39, s39, 4096\n"
"buffer_load_dwordx4 v[160:163], v51, s[4:7], s39 offen offset:0 sc1\n"
"buffer_load_dwordx4 v[164:167], v51, s[4:7], s39 offen offset:1024 sc1\n"
"ds_read_b128 v[64:67], v2 offset:0\n"
"ds_read_b128 v[68:71], v2 offset:1024\n"
"ds_read_b128 v[72:75], v2 offset:2048\n"
"ds_read_b128 v[76:79], v2 offset:3072\n"
"ds_read_b128 v[80:83], v2 offset:4096\n"
"ds_read_b128 v[84:87], v2 offset:5120\n"
"s_waitcnt vmcnt(9)\n"
"v_permlane16_swap_b32 v128, v129\n"
"v_permlane16_swap_b32 v130, v131\n"
"v_mov_b32 v20, v128\n"
"v_mov_b32_e64 v21, v129\n"
"v_mov_b32_dpp v128, v130 row_ror:8 row_mask:0xf bank_mask:0xc\n"
"v_mov_b32_dpp v129, v131 row_ror:8 row_mask:0xf bank_mask:0xc\n"
"v_mov_b32_dpp v130, v20 row_ror:8 row_mask:0xf bank_mask:0x3\n"
"v_mov_b32_dpp v131, v21 row_ror:8 row_mask:0xf bank_mask:0x3\n"
"s_waitcnt vmcnt(8) lgkmcnt(5)\n"
"v_permlane16_swap_b32 v132, v133\n"
"v_permlane16_swap_b32 v134, v135\n"
"v_mov_b32 v22, v132\n"
"v_mov_b32_e64 v23, v133\n"
"v_mov_b32_dpp v132, v134 row_ror:8 row_mask:0xf bank_mask:0xc\n"
"v_mov_b32_dpp v133, v135 row_ror:8 row_mask:0xf bank_mask:0xc\n"
"v_mov_b32_dpp v134, v22 row_ror:8 row_mask:0xf bank_mask:0x3\n"
"v_mov_b32_dpp v135, v23 row_ror:8 row_mask:0xf bank_mask:0x3\n"
"v_mfma_f32_32x32x16_bf16 v[96:111], v[64:67], v[128:131], 0\n"
"ds_read_b128 v[88:91], v2 offset:6144\n"
"buffer_load_dwordx4 v[168:171], v51, s[4:7], s39 offen offset:2048 sc1\n"
"s_waitcnt vmcnt(8) lgkmcnt(5)\n"
"v_permlane16_swap_b32 v136, v137\n"
"v_permlane16_swap_b32 v138, v139\n"
"v_mov_b32 v20, v136\n"
"v_mov_b32_e64 v21, v137\n"
"v_mfma_f32_16x16x32_bf16 a[240:243], a[244:247], v[128:131], 0\n"
"v_mov_b32_dpp v136, v138 row_ror:8 row_mask:0xf bank_mask:0xc\n"
"v_mov_b32_dpp v137, v139 row_ror:8 row_mask:0xf bank_mask:0xc\n"
"v_mov_b32_dpp v138, v20 row_ror:8 row_mask:0xf bank_mask:0x3\n"
"v_mov_b32_dpp v139, v21 row_ror:8 row_mask:0xf bank_mask:0x3\n"
"v_mfma_f32_32x32x16_bf16 v[96:111], v[68:71], v[132:135], v[96:111]\n"
"ds_read_b128 v[92:95], v2 offset:7168\n"
"buffer_load_dwordx4 v[172:175], v51, s[4:7], s39 offen offset:3072 sc1\n"
"s_add_u32 s39, s39, 4096\n"
"s_waitcnt vmcnt(8) lgkmcnt(5)\n"
"v_permlane16_swap_b32 v140, v141\n"
"v_permlane16_swap_b32 v142, v143\n"
"v_mov_b32 v22, v140\n"
"v_mov_b32_e64 v23, v141\n"
"v_mfma_f32_16x16x32_bf16 a[240:243], a[244:247], v[132:135], a[240:243]\n"
"v_mov_b32_dpp v140, v142 row_ror:8 row_mask:0xf bank_mask:0xc\n"
"v_mov_b32_dpp v141, v143 row_ror:8 row_mask:0xf bank_mask:0xc\n"
"v_mov_b32_dpp v142, v22 row_ror:8 row_mask:0xf bank_mask:0x3\n"
"v_mov_b32_dpp v143, v23 row_ror:8 row_mask:0xf bank_mask:0x3\n"
"v_mfma_f32_32x32x16_bf16 v[96:111], v[72:75], v[136:139], v[96:111]\n"
"ds_read_b128 v[64:67], v2 offset:8192\n"
"buffer_load_dwordx4 v[176:179], v51, s[4:7], s39 offen offset:0 sc1\n"
"s_waitcnt vmcnt(8) lgkmcnt(5)\n"
"v_permlane16_swap_b32 v144, v145\n"
"v_permlane16_swap_b32 v146, v147\n"
"v_mov_b32 v20, v144\n"
"v_mov_b32_e64 v21, v145\n"
"v_mfma_f32_16x16x32_bf16 a[240:243], a[244:247], v[136:139], a[240:243]\n"
"v_mov_b32_dpp v144, v146 row_ror:8 row_mask:0xf bank_mask:0xc\n"
"v_mov_b32_dpp v145, v147 row_ror:8 row_mask:0xf bank_mask:0xc\n"
"v_mov_b32_dpp v146, v20 row_ror:8 row_mask:0xf bank_mask:0x3\n"
"v_mov_b32_dpp v147, v21 row_ror:8 row_mask:0xf bank_mask:0x3\n"
"v_mfma_f32_32x32x16_bf16 v[96:111], v[76:79], v[140:143], v[96:111]\n"
"ds_read_b128 v[68:71], v2 offset:9216\n"
"buffer_load_dwordx4 v[180:183], v51, s[4:7], s39 offen offset:1024 sc1\n"
"s_waitcnt vmcnt(8) lgkmcnt(5)\n"
"v_permlane16_swap_b32 v148, v149\n"
"v_permlane16_swap_b32 v150, v151\n"
"v_mov_b32 v22, v148\n"
"v_mov_b32_e64 v23, v149\n"
"v_mfma_f32_16x16x32_bf16 a[240:243], a[244:247], v[140:143], a[240:243]\n"
"v_mov_b32_dpp v148, v150 row_ror:8 row_mask:0xf bank_mask:0xc\n"
"v_mov_b32_dpp v149, v151 row_ror:8 row_mask:0xf bank_mask:0xc\n"
"v_mov_b32_dpp v150, v22 row_ror:8 row_mask:0xf bank_mask:0x3\n"
"v_mov_b32_dpp v151, v23 row_ror:8 row_mask:0xf bank_mask:0x3\n"
"v_mfma_f32_32x32x16_bf16 v[96:111], v[80:83], v[144:147], v[96:111]\n"
"ds_read_b128 v[72:75], v2 offset:10240\n"
"buffer_load_dwordx4 v[184:187], v51, s[4:7], s39 offen offset:2048 sc1\n"
"s_waitcnt vmcnt(8) lgkmcnt(5)\n"
"v_permlane16_swap_b32 v152, v153\n"
"v_permlane16_swap_b32 v154, v155\n"
"v_mov_b32 v20, v152\n"
"v_mov_b32_e64 v21, v153\n"
"v_mfma_f32_16x16x32_bf16 a[240:243], a[244:247], v[144:147], a[240:243]\n"
"v_mov_b32_dpp v152, v154 row_ror:8 row_mask:0xf bank_mask:0xc\n"
"v_mov_b32_dpp v153, v155 row_ror:8 row_mask:0xf bank_mask:0xc\n"
"v_mov_b32_dpp v154, v20 row_ror:8 row_mask:0xf bank_mask:0x3\n"
"v_mov_b32_dpp v155, v21 row_ror:8 row_mask:0xf bank_mask:0x3\n"
"v_mfma_f32_32x32x16_bf16 v[96:111], v[84:87], v[148:151], v[96:111]\n"
"ds_read_b128 v[76:79], v2 offset:11264\n"
"buffer_load_dwordx4 v[188:191], v51, s[4:7], s39 offen offset:3072 sc1\n"
"s_add_u32 s39, s39, 4096\n"
"s_waitcnt vmcnt(8) lgkmcnt(5)\n"
"v_permlane16_swap_b32 v156, v157\n"
"v_permlane16_swap_b32 v158, v159\n"
"v_mov_b32 v22, v156\n"
"v_mov_b32_e64 v23, v157\n"
"v_mfma_f32_16x16x32_bf16 a[240:243], a[244:247], v[148:151], a[240:243]\n"
"v_mov_b32_dpp v156, v158 row_ror:8 row_mask:0xf bank_mask:0xc\n"
"v_mov_b32_dpp v157, v159 row_ror:8 row_mask:0xf bank_mask:0xc\n"
"v_mov_b32_dpp v158, v22 row_ror:8 row_mask:0xf bank_mask:0x3\n"
"v_mov_b32_dpp v159, v23 row_ror:8 row_mask:0xf bank_mask:0x3\n"
"v_mfma_f32_32x32x16_bf16 v[96:111], v[88:91], v[152:155], v[96:111]\n"
"ds_read_b128 v[80:83], v2 offset:12288\n"
"buffer_load_dwordx4 v[192:195], v51, s[4:7], s39 offen offset:0 sc1\n"
"s_waitcnt vmcnt(8) lgkmcnt(5)\n"
"v_permlane16_swap_b32 v160, v161\n"
"v_permlane16_swap_b32 v162, v163\n"
"v_mov_b32 v20, v160\n"
"v_mov_b32_e64 v21, v161\n"
"v_mfma_f32_16x16x32_bf16 a[240:243], a[244:247], v[152:155], a[240:243]\n"
"v_mov_b32_dpp v160, v162 row_ror:8 row_mask:0xf bank_mask:0xc\n"
"v_mov_b32_dpp v161, v163 row_ror:8 row_mask:0xf bank_mask:0xc\n"
"v_mov_b32_dpp v162, v20 row_ror:8 row_mask:0xf bank_mask:0x3\n"
"v_mov_b32_dpp v163, v21 row_ror:8 row_mask:0xf bank_mask:0x3\n"
"v_mfma_f32_32x32x16_bf16 v[96:111], v[92:95], v[156:159], v[96:111]\n"
"ds_read_b128 v[84:87], v2 offset:13312\n"
"buffer_load_dwordx4 v[196:199], v51, s[4:7], s39 offen offset:1024 sc1\n"
"s_waitcnt vmcnt(8) lgkmcnt(5)\n"
"v_permlane16_swap_b32 v164, v165\n"
"v_permlane16_swap_b32 v166, v167\n"
"v_mov_b32 v22, v164\n"
"v_mov_b32_e64 v23, v165\n"
"v_mfma_f32_16x16x32_bf16 a[240:243], a[244:247], v[156:159], a[240:243]\n"
"v_mov_b32_dpp v164, v166 row_ror:8 row_mask:0xf bank_mask:0xc\n"
"v_mov_b32_dpp v165, v167 row_ror:8 row_mask:0xf bank_mask:0xc\n"
"v_mov_b32_dpp v166, v22 row_ror:8 row_mask:0xf bank_mask:0x3\n"
"v_mov_b32_dpp v167, v23 row_ror:8 row_mask:0xf bank_mask:0x3\n"
"v_mfma_f32_32x32x16_bf16 v[96:111], v[64:67], v[160:163], v[96:111]\n"
"ds_read_b128 v[88:91], v2 offset:14336\n"
"buffer_load_dwordx4 v[200:203], v51, s[4:7], s39 offen offset:2048 sc1\n"
"s_waitcnt vmcnt(8) lgkmcnt(5)\n"
"v_permlane16_swap_b32 v168, v169\n"
"v_permlane16_swap_b32 v170, v171\n"
"v_mov_b32 v20, v168\n"
"v_mov_b32_e64 v21, v169\n"
"v_mfma_f32_16x16x32_bf16 a[240:243], a[244:247], v[160:163], a[240:243]\n"
"v_mov_b32_dpp v168, v170 row_ror:8 row_mask:0xf bank_mask:0xc\n"
"v_mov_b32_dpp v169, v171 row_ror:8 row_mask:0xf bank_mask:0xc\n"
"v_mov_b32_dpp v170, v20 row_ror:8 row_mask:0xf bank_mask:0x3\n"
"v_mov_b32_dpp v171, v21 row_ror:8 row_mask:0xf bank_mask:0x3\n"
"v_mfma_f32_32x32x16_bf16 v[96:111], v[68:71], v[164:167], v[96:111]\n"
"ds_read_b128 v[92:95], v2 offset:15360\n"
"buffer_load_dwordx4 v[204:207], v51, s[4:7], s39 offen offset:3072 sc1\n"
"s_add_u32 s39, s39, 4096\n"
"s_waitcnt vmcnt(8) lgkmcnt(5)\n"
"v_permlane16_swap_b32 v172, v173\n"
"v_permlane16_swap_b32 v174, v175\n"
"v_mov_b32 v22, v172\n"
"v_mov_b32_e64 v23, v173\n"
"v_mfma_f32_16x16x32_bf16 a[240:243], a[244:247], v[164:167], a[240:243]\n"
"v_mov_b32_dpp v172, v174 row_ror:8 row_mask:0xf bank_mask:0xc\n"
"v_mov_b32_dpp v173, v175 row_ror:8 row_mask:0xf bank_mask:0xc\n"
"v_mov_b32_dpp v174, v22 row_ror:8 row_mask:0xf bank_mask:0x3\n"
"v_mov_b32_dpp v175, v23 row_ror:8 row_mask:0xf bank_mask:0x3\n"
"v_mfma_f32_32x32x16_bf16 v[96:111], v[72:75], v[168:171], v[96:111]\n"
"ds_read_b128 v[64:67], v2 offset:16384\n"
"buffer_load_dwordx4 v[208:211], v51, s[4:7], s39 offen offset:0 sc1\n"
"s_waitcnt vmcnt(8) lgkmcnt(5)\n"
"v_permlane16_swap_b32 v176, v177\n"
"v_permlane16_swap_b32 v178, v179\n"
"v_mov_b32 v20, v176\n"
"v_mov_b32_e64 v21, v177\n"
"v_mfma_f32_16x16x32_bf16 a[240:243], a[244:247], v[168:171], a[240:243]\n"
"v_mov_b32_dpp v176, v178 row_ror:8 row_mask:0xf bank_mask:0xc\n"
"v_mov_b32_dpp v177, v179 row_ror:8 row_mask:0xf bank_mask:0xc\n"
"v_mov_b32_dpp v178, v20 row_ror:8 row_mask:0xf bank_mask:0x3\n"
"v_mov_b32_dpp v179, v21 row_ror:8 row_mask:0xf bank_mask:0x3\n"
"v_mfma_f32_32x32x16_bf16 v[96:111], v[76:79], v[172:175], v[96:111]\n"
"ds_read_b128 v[68:71], v2 offset:17408\n"
"buffer_load_dwordx4 v[212:215], v51, s[4:7], s39 offen offset:1024 sc1\n"
"s_waitcnt vmcnt(8) lgkmcnt(5)\n"
"v_permlane16_swap_b32 v180, v181\n"
"v_permlane16_swap_b32 v182, v183\n"
"v_mov_b32 v22, v180\n"
"v_mov_b32_e64 v23, v181\n"
"v_mfma_f32_16x16x32_bf16 a[240:243], a[244:247], v[172:175], a[240:243]\n"
"v_mov_b32_dpp v180, v182 row_ror:8 row_mask:0xf bank_mask:0xc\n"
"v_mov_b32_dpp v181, v183 row_ror:8 row_mask:0xf bank_mask:0xc\n"
"v_mov_b32_dpp v182, v22 row_ror:8 row_mask:0xf bank_mask:0x3\n"
"v_mov_b32_dpp v183, v23 row_ror:8 row_mask:0xf bank_mask:0x3\n"
"v_mfma_f32_32x32x16_bf16 v[96:111], v[80:83], v[176:179], v[96:111]\n"
"ds_read_b128 v[72:75], v2 offset:18432\n"
"buffer_load_dwordx4 v[216:219], v51, s[4:7], s39 offen offset:2048 sc1\n"
"s_waitcnt vmcnt(8) lgkmcnt(5)\n"
"v_permlane16_swap_b32 v184, v185\n"
"v_permlane16_swap_b32 v186, v187\n"
"v_mov_b32 v20, v184\n"
"v_mov_b32_e64 v21, v185\n"
"v_mfma_f32_16x16x32_bf16 a[240:243], a[244:247], v[176:179], a[240:243]\n"
"v_mov_b32_dpp v184, v186 row_ror:8 row_mask:0xf bank_mask:0xc\n"
"v_mov_b32_dpp v185, v187 row_ror:8 row_mask:0xf bank_mask:0xc\n"
"v_mov_b32_dpp v186, v20 row_ror:8 row_mask:0xf bank_mask:0x3\n"
"v_mov_b32_dpp v187, v21 row_ror:8 row_mask:0xf bank_mask:0x3\n"
"v_mfma_f32_32x32x16_bf16 v[96:111], v[84:87], v[180:183], v[96:111]\n"
"ds_read_b128 v[76:79], v2 offset:19456\n"
"buffer_load_dwordx4 v[220:223], v51, s[4:7], s39 offen offset:3072 sc1\n"
"s_add_u32 s39, s39, 4096\n"
"s_waitcnt vmcnt(8) lgkmcnt(5)\n"
"v_permlane16_swap_b32 v188, v189\n"
"v_permlane16_swap_b32 v190, v191\n"
"v_mov_b32 v22, v188\n"
"v_mov_b32_e64 v23, v189\n"
"v_mfma_f32_16x16x32_bf16 a[240:243], a[244:247], v[180:183], a[240:243]\n"
"v_mov_b32_dpp v188, v190 row_ror:8 row_mask:0xf bank_mask:0xc\n"
"v_mov_b32_dpp v189, v191 row_ror:8 row_mask:0xf bank_mask:0xc\n"
"v_mov_b32_dpp v190, v22 row_ror:8 row_mask:0xf bank_mask:0x3\n"
"v_mov_b32_dpp v191, v23 row_ror:8 row_mask:0xf bank_mask:0x3\n"
"v_mfma_f32_32x32x16_bf16 v[96:111], v[88:91], v[184:187], v[96:111]\n"
"ds_read_b128 v[80:83], v2 offset:20480\n"
"buffer_load_dwordx4 v[224:227], v51, s[4:7], s39 offen offset:0 sc1\n"
"s_waitcnt vmcnt(8) lgkmcnt(5)\n"
"v_permlane16_swap_b32 v192, v193\n"
"v_permlane16_swap_b32 v194, v195\n"
"v_mov_b32 v20, v192\n"
"v_mov_b32_e64 v21, v193\n"
"v_mfma_f32_16x16x32_bf16 a[240:243], a[244:247], v[184:187], a[240:243]\n"
"v_mov_b32_dpp v192, v194 row_ror:8 row_mask:0xf bank_mask:0xc\n"
"v_mov_b32_dpp v193, v195 row_ror:8 row_mask:0xf bank_mask:0xc\n"
"v_mov_b32_dpp v194, v20 row_ror:8 row_mask:0xf bank_mask:0x3\n"
"v_mov_b32_dpp v195, v21 row_ror:8 row_mask:0xf bank_mask:0x3\n"
"v_mfma_f32_32x32x16_bf16 v[96:111], v[92:95], v[188:191], v[96:111]\n"
"ds_read_b128 v[84:87], v2 offset:21504\n"
"buffer_load_dwordx4 v[228:231], v51, s[4:7], s39 offen offset:1024 sc1\n"
"s_waitcnt vmcnt(8) lgkmcnt(5)\n"
"v_permlane16_swap_b32 v196, v197\n"
"v_permlane16_swap_b32 v198, v199\n"
"v_mov_b32 v22, v196\n"
"v_mov_b32_e64 v23, v197\n"
"v_mfma_f32_16x16x32_bf16 a[240:243], a[244:247], v[188:191], a[240:243]\n"
"v_mov_b32_dpp v196, v198 row_ror:8 row_mask:0xf bank_mask:0xc\n"
"v_mov_b32_dpp v197, v199 row_ror:8 row_mask:0xf bank_mask:0xc\n"
"v_mov_b32_dpp v198, v22 row_ror:8 row_mask:0xf bank_mask:0x3\n"
"v_mov_b32_dpp v199, v23 row_ror:8 row_mask:0xf bank_mask:0x3\n"
"v_mfma_f32_32x32x16_bf16 v[96:111], v[64:67], v[192:195], v[96:111]\n"
"ds_read_b128 v[88:91], v2 offset:22528\n"
"buffer_load_dwordx4 v[232:235], v51, s[4:7], s39 offen offset:2048 sc1\n"
"s_waitcnt vmcnt(8) lgkmcnt(5)\n"
"v_permlane16_swap_b32 v200, v201\n"
"v_permlane16_swap_b32 v202, v203\n"
"v_mov_b32 v20, v200\n"
"v_mov_b32_e64 v21, v201\n"
"v_mfma_f32_16x16x32_bf16 a[240:243], a[244:247], v[192:195], a[240:243]\n"
"v_mov_b32_dpp v200, v202 row_ror:8 row_mask:0xf bank_mask:0xc\n"
"v_mov_b32_dpp v201, v203 row_ror:8 row_mask:0xf bank_mask:0xc\n"
"v_mov_b32_dpp v202, v20 row_ror:8 row_mask:0xf bank_mask:0x3\n"
"v_mov_b32_dpp v203, v21 row_ror:8 row_mask:0xf bank_mask:0x3\n"
"v_mfma_f32_32x32x16_bf16 v[96:111], v[68:71], v[196:199], v[96:111]\n"
"ds_read_b128 v[92:95], v2 offset:23552\n"
"buffer_load_dwordx4 v[236:239], v51, s[4:7], s39 offen offset:3072 sc1\n"
"s_add_u32 s39, s39, 4096\n"
"s_waitcnt vmcnt(8) lgkmcnt(5)\n"
"v_permlane16_swap_b32 v204, v205\n"
"v_permlane16_swap_b32 v206, v207\n"
"v_mov_b32 v22, v204\n"
"v_mov_b32_e64 v23, v205\n"
"v_mfma_f32_16x16x32_bf16 a[240:243], a[244:247], v[196:199], a[240:243]\n"
"v_mov_b32_dpp v204, v206 row_ror:8 row_mask:0xf bank_mask:0xc\n"
"v_mov_b32_dpp v205, v207 row_ror:8 row_mask:0xf bank_mask:0xc\n"
"v_mov_b32_dpp v206, v22 row_ror:8 row_mask:0xf bank_mask:0x3\n"
"v_mov_b32_dpp v207, v23 row_ror:8 row_mask:0xf bank_mask:0x3\n"
"v_mfma_f32_32x32x16_bf16 v[96:111], v[72:75], v[200:203], v[96:111]\n"
"ds_read_b128 v[64:67], v2 offset:24576\n"
"buffer_load_dwordx4 v[240:243], v51, s[4:7], s39 offen offset:0 sc1\n"
"s_waitcnt vmcnt(8) lgkmcnt(5)\n"
"v_permlane16_swap_b32 v208, v209\n"
"v_permlane16_swap_b32 v210, v211\n"
"v_mov_b32 v20, v208\n"
"v_mov_b32_e64 v21, v209\n"
"v_mfma_f32_16x16x32_bf16 a[240:243], a[244:247], v[200:203], a[240:243]\n"
"v_mov_b32_dpp v208, v210 row_ror:8 row_mask:0xf bank_mask:0xc\n"
"v_mov_b32_dpp v209, v211 row_ror:8 row_mask:0xf bank_mask:0xc\n"
"v_mov_b32_dpp v210, v20 row_ror:8 row_mask:0xf bank_mask:0x3\n"
"v_mov_b32_dpp v211, v21 row_ror:8 row_mask:0xf bank_mask:0x3\n"
"v_mfma_f32_32x32x16_bf16 v[96:111], v[76:79], v[204:207], v[96:111]\n"
"ds_read_b128 v[68:71], v2 offset:25600\n"
"buffer_load_dwordx4 v[244:247], v51, s[4:7], s39 offen offset:1024 sc1\n"
"s_waitcnt vmcnt(8) lgkmcnt(5)\n"
"v_permlane16_swap_b32 v212, v213\n"
"v_permlane16_swap_b32 v214, v215\n"
"v_mov_b32 v22, v212\n"
"v_mov_b32_e64 v23, v213\n"
"v_mfma_f32_16x16x32_bf16 a[240:243], a[244:247], v[204:207], a[240:243]\n"
"v_mov_b32_dpp v212, v214 row_ror:8 row_mask:0xf bank_mask:0xc\n"
"v_mov_b32_dpp v213, v215 row_ror:8 row_mask:0xf bank_mask:0xc\n"
"v_mov_b32_dpp v214, v22 row_ror:8 row_mask:0xf bank_mask:0x3\n"
"v_mov_b32_dpp v215, v23 row_ror:8 row_mask:0xf bank_mask:0x3\n"
"v_mfma_f32_32x32x16_bf16 v[96:111], v[80:83], v[208:211], v[96:111]\n"
"ds_read_b128 v[72:75], v2 offset:26624\n"
"buffer_load_dwordx4 v[248:251], v51, s[4:7], s39 offen offset:2048 sc1\n"
"s_waitcnt vmcnt(8) lgkmcnt(5)\n"
"v_permlane16_swap_b32 v216, v217\n"
"v_permlane16_swap_b32 v218, v219\n"
"v_mov_b32 v20, v216\n"
"v_mov_b32_e64 v21, v217\n"
"v_mfma_f32_16x16x32_bf16 a[240:243], a[244:247], v[208:211], a[240:243]\n"
"v_mov_b32_dpp v216, v218 row_ror:8 row_mask:0xf bank_mask:0xc\n"
"v_mov_b32_dpp v217, v219 row_ror:8 row_mask:0xf bank_mask:0xc\n"
"v_mov_b32_dpp v218, v20 row_ror:8 row_mask:0xf bank_mask:0x3\n"
"v_mov_b32_dpp v219, v21 row_ror:8 row_mask:0xf bank_mask:0x3\n"
"v_mfma_f32_32x32x16_bf16 v[96:111], v[84:87], v[212:215], v[96:111]\n"
"ds_read_b128 v[76:79], v2 offset:27648\n"
"buffer_load_dwordx4 v[252:255], v51, s[4:7], s39 offen offset:3072 sc1\n"
"s_waitcnt vmcnt(8) lgkmcnt(5)\n"
"v_permlane16_swap_b32 v220, v221\n"
"v_permlane16_swap_b32 v222, v223\n"
"v_mov_b32 v22, v220\n"
"v_mov_b32_e64 v23, v221\n"
"v_mfma_f32_16x16x32_bf16 a[240:243], a[244:247], v[212:215], a[240:243]\n"
"v_mov_b32_dpp v220, v222 row_ror:8 row_mask:0xf bank_mask:0xc\n"
"v_mov_b32_dpp v221, v223 row_ror:8 row_mask:0xf bank_mask:0xc\n"
"v_mov_b32_dpp v222, v22 row_ror:8 row_mask:0xf bank_mask:0x3\n"
"v_mov_b32_dpp v223, v23 row_ror:8 row_mask:0xf bank_mask:0x3\n"
"v_mfma_f32_32x32x16_bf16 v[96:111], v[88:91], v[216:219], v[96:111]\n"
"ds_read_b128 v[80:83], v2 offset:28672\n"
"buffer_load_dwordx4 a[0:3], v42, s[4:7], s71 offen offset:0 sc1\n"
"s_waitcnt vmcnt(8) lgkmcnt(5)\n"
"v_permlane16_swap_b32 v224, v225\n"
"v_permlane16_swap_b32 v226, v227\n"
"v_mov_b32 v20, v224\n"
"v_mov_b32_e64 v21, v225\n"
"v_mfma_f32_16x16x32_bf16 a[240:243], a[244:247], v[216:219], a[240:243]\n"
"v_mov_b32_dpp v224, v226 row_ror:8 row_mask:0xf bank_mask:0xc\n"
"v_mov_b32_dpp v225, v227 row_ror:8 row_mask:0xf bank_mask:0xc\n"
"v_mov_b32_dpp v226, v20 row_ror:8 row_mask:0xf bank_mask:0x3\n"
"v_mov_b32_dpp v227, v21 row_ror:8 row_mask:0xf bank_mask:0x3\n"
"v_mfma_f32_32x32x16_bf16 v[96:111], v[92:95], v[220:223], v[96:111]\n"
"ds_read_b128 v[84:87], v2 offset:29696\n"
"buffer_load_dwordx4 a[4:7], v43, s[4:7], s71 offen offset:0 sc1\n"
"s_waitcnt vmcnt(8) lgkmcnt(5)\n"
"v_permlane16_swap_b32 v228, v229\n"
"v_permlane16_swap_b32 v230, v231\n"
"v_mov_b32 v22, v228\n"
"v_mov_b32_e64 v23, v229\n"
"v_mfma_f32_16x16x32_bf16 a[240:243], a[244:247], v[220:223], a[240:243]\n"
"v_mov_b32_dpp v228, v230 row_ror:8 row_mask:0xf bank_mask:0xc\n"
"v_mov_b32_dpp v229, v231 row_ror:8 row_mask:0xf bank_mask:0xc\n"
"v_mov_b32_dpp v230, v22 row_ror:8 row_mask:0xf bank_mask:0x3\n"
"v_mov_b32_dpp v231, v23 row_ror:8 row_mask:0xf bank_mask:0x3\n"
"v_mfma_f32_32x32x16_bf16 v[96:111], v[64:67], v[224:227], v[96:111]\n"
"ds_read_b128 v[88:91], v2 offset:30720\n"
"buffer_load_dwordx4 a[8:11], v42, s[4:7], s73 offen offset:0 sc1\n"
"s_waitcnt vmcnt(8) lgkmcnt(5)\n"
"v_permlane16_swap_b32 v232, v233\n"
"v_permlane16_swap_b32 v234, v235\n"
"v_mov_b32 v20, v232\n"
"v_mov_b32_e64 v21, v233\n"
"v_mfma_f32_16x16x32_bf16 a[240:243], a[244:247], v[224:227], a[240:243]\n"
"v_mov_b32_dpp v232, v234 row_ror:8 row_mask:0xf bank_mask:0xc\n"
"v_mov_b32_dpp v233, v235 row_ror:8 row_mask:0xf bank_mask:0xc\n"
"v_mov_b32_dpp v234, v20 row_ror:8 row_mask:0xf bank_mask:0x3\n"
"v_mov_b32_dpp v235, v21 row_ror:8 row_mask:0xf bank_mask:0x3\n"
"v_mfma_f32_32x32x16_bf16 v[96:111], v[68:71], v[228:231], v[96:111]\n"
"ds_read_b128 v[92:95], v2 offset:31744\n"
"buffer_load_dwordx4 a[12:15], v43, s[4:7], s73 offen offset:0 sc1\n"
"s_waitcnt vmcnt(8) lgkmcnt(5)\n"
"v_permlane16_swap_b32 v236, v237\n"
"v_permlane16_swap_b32 v238, v239\n"
"v_mov_b32 v22, v236\n"
"v_mov_b32_e64 v23, v237\n"
"v_mfma_f32_16x16x32_bf16 a[240:243], a[244:247], v[228:231], a[240:243]\n"
"v_mov_b32_dpp v236, v238 row_ror:8 row_mask:0xf bank_mask:0xc\n"
"v_mov_b32_dpp v237, v239 row_ror:8 row_mask:0xf bank_mask:0xc\n"
"v_mov_b32_dpp v238, v22 row_ror:8 row_mask:0xf bank_mask:0x3\n"
"v_mov_b32_dpp v239, v23 row_ror:8 row_mask:0xf bank_mask:0x3\n"
"v_mfma_f32_32x32x16_bf16 v[96:111], v[72:75], v[232:235], v[96:111]\n"
"ds_read_b128 v[64:67], v2 offset:32768\n"
"buffer_load_dwordx4 a[16:19], v42, s[4:7], s75 offen offset:0 sc1\n"
"s_waitcnt vmcnt(8) lgkmcnt(5)\n"
"v_permlane16_swap_b32 v240, v241\n"
"v_permlane16_swap_b32 v242, v243\n"
"v_mov_b32 v20, v240\n"
"v_mov_b32_e64 v21, v241\n"
"v_mfma_f32_16x16x32_bf16 a[240:243], a[244:247], v[232:235], a[240:243]\n"
"v_mov_b32_dpp v240, v242 row_ror:8 row_mask:0xf bank_mask:0xc\n"
"v_mov_b32_dpp v241, v243 row_ror:8 row_mask:0xf bank_mask:0xc\n"
"v_mov_b32_dpp v242, v20 row_ror:8 row_mask:0xf bank_mask:0x3\n"
"v_mov_b32_dpp v243, v21 row_ror:8 row_mask:0xf bank_mask:0x3\n"
"v_mfma_f32_32x32x16_bf16 v[96:111], v[76:79], v[236:239], v[96:111]\n"
"ds_read_b128 v[68:71], v2 offset:33792\n"
"buffer_load_dwordx4 a[20:23], v43, s[4:7], s75 offen offset:0 sc1\n"
"s_waitcnt vmcnt(8) lgkmcnt(5)\n"
"v_permlane16_swap_b32 v244, v245\n"
"v_permlane16_swap_b32 v246, v247\n"
"v_mov_b32 v22, v244\n"
"v_mov_b32_e64 v23, v245\n"
"v_mfma_f32_16x16x32_bf16 a[240:243], a[244:247], v[236:239], a[240:243]\n"
"v_mov_b32_dpp v244, v246 row_ror:8 row_mask:0xf bank_mask:0xc\n"
"v_mov_b32_dpp v245, v247 row_ror:8 row_mask:0xf bank_mask:0xc\n"
"v_mov_b32_dpp v246, v22 row_ror:8 row_mask:0xf bank_mask:0x3\n"
"v_mov_b32_dpp v247, v23 row_ror:8 row_mask:0xf bank_mask:0x3\n"
"v_mfma_f32_32x32x16_bf16 v[96:111], v[80:83], v[240:243], v[96:111]\n"
"ds_read_b128 v[72:75], v2 offset:34816\n"
"buffer_load_dwordx4 a[24:27], v42, s[4:7], s77 offen offset:0 sc1\n"
"s_waitcnt vmcnt(8) lgkmcnt(5)\n"
"v_permlane16_swap_b32 v248, v249\n"
"v_permlane16_swap_b32 v250, v251\n"
"v_mov_b32 v20, v248\n"
"v_mov_b32_e64 v21, v249\n"
"v_mfma_f32_16x16x32_bf16 a[240:243], a[244:247], v[240:243], a[240:243]\n"
"v_mov_b32_dpp v248, v250 row_ror:8 row_mask:0xf bank_mask:0xc\n"
"v_mov_b32_dpp v249, v251 row_ror:8 row_mask:0xf bank_mask:0xc\n"
"v_mov_b32_dpp v250, v20 row_ror:8 row_mask:0xf bank_mask:0x3\n"
"v_mov_b32_dpp v251, v21 row_ror:8 row_mask:0xf bank_mask:0x3\n"
"v_mfma_f32_32x32x16_bf16 v[96:111], v[84:87], v[244:247], v[96:111]\n"
"ds_read_b128 v[76:79], v2 offset:35840\n"
"buffer_load_dwordx4 a[28:31], v43, s[4:7], s77 offen offset:0 sc1\n"
"s_waitcnt vmcnt(8) lgkmcnt(5)\n"
"v_permlane16_swap_b32 v252, v253\n"
"v_permlane16_swap_b32 v254, v255\n"
"v_mov_b32 v22, v252\n"
"v_mov_b32_e64 v23, v253\n"
"v_mfma_f32_16x16x32_bf16 a[240:243], a[244:247], v[244:247], a[240:243]\n"
"v_mov_b32_dpp v252, v254 row_ror:8 row_mask:0xf bank_mask:0xc\n"
"v_mov_b32_dpp v253, v255 row_ror:8 row_mask:0xf bank_mask:0xc\n"
"v_mov_b32_dpp v254, v22 row_ror:8 row_mask:0xf bank_mask:0x3\n"
"v_mov_b32_dpp v255, v23 row_ror:8 row_mask:0xf bank_mask:0x3\n"
"v_mfma_f32_32x32x16_bf16 v[96:111], v[88:91], v[248:251], v[96:111]\n"
"ds_read_b128 v[80:83], v2 offset:36864\n"
"buffer_load_dwordx4 a[32:35], v42, s[4:7], s71 offen offset:1024 sc1\n"
"s_waitcnt lgkmcnt(4)\n"
"v_mfma_f32_16x16x32_bf16 a[240:243], a[244:247], v[248:251], a[240:243]\n"
"v_mfma_f32_32x32x16_bf16 v[96:111], v[92:95], v[252:255], v[96:111]\n"
"ds_read_b128 v[84:87], v2 offset:37888\n"
"buffer_load_dwordx4 a[36:39], v43, s[4:7], s71 offen offset:1024 sc1\n"
"s_waitcnt lgkmcnt(4)\n"
"v_mfma_f32_16x16x32_bf16 a[240:243], a[244:247], v[252:255], a[240:243]\n"
"v_mov_b32 v20, 0\n"
"v_mov_b32 v21, 0\n"
"v_mov_b32 v32, -1\n"
"v_mov_b32 v33, -1\n"
"v_mov_b32 v34, -1\n"
"v_mov_b32 v35, -1\n"
"v_dot2c_f32_bf16 v20, v128, v128\n"
"v_dot2c_f32_bf16 v21, v129, v129\n"
"v_dot2c_f32_bf16 v20, v130, v130\n"
"v_dot2c_f32_bf16 v21, v131, v131\n"
"v_dot2c_f32_bf16 v20, v132, v132\n"
"v_dot2c_f32_bf16 v21, v133, v133\n"
"v_dot2c_f32_bf16 v20, v134, v134\n"
"v_dot2c_f32_bf16 v21, v135, v135\n"
"v_dot2c_f32_bf16 v20, v136, v136\n"
"v_dot2c_f32_bf16 v21, v137, v137\n"
"v_dot2c_f32_bf16 v20, v138, v138\n"
"buffer_load_dwordx4 a[40:43], v42, s[4:7], s73 offen offset:1024 sc1\n"
"v_dot2c_f32_bf16 v21, v139, v139\n"
"v_dot2c_f32_bf16 v20, v140, v140\n"
"v_dot2c_f32_bf16 v21, v141, v141\n"
"v_dot2c_f32_bf16 v20, v142, v142\n"
"v_dot2c_f32_bf16 v21, v143, v143\n"
"v_dot2c_f32_bf16 v20, v144, v144\n"
"v_dot2c_f32_bf16 v21, v145, v145\n"
"v_dot2c_f32_bf16 v20, v146, v146\n"
"v_dot2c_f32_bf16 v21, v147, v147\n"
"v_dot2c_f32_bf16 v20, v148, v148\n"
"v_dot2c_f32_bf16 v21, v149, v149\n"
"v_dot2c_f32_bf16 v20, v150, v150\n"
"v_dot2c_f32_bf16 v21, v151, v151\n"
"v_dot2c_f32_bf16 v20, v152, v152\n"
"v_dot2c_f32_bf16 v21, v153, v153\n"
"v_dot2c_f32_bf16 v20, v154, v154\n"
"v_dot2c_f32_bf16 v21, v155, v155\n"
"v_dot2c_f32_bf16 v20, v156, v156\n"
"v_dot2c_f32_bf16 v21, v157, v157\n"
"v_dot2c_f32_bf16 v20, v158, v158\n"
"buffer_load_dwordx4 a[44:47], v43, s[4:7], s73 offen offset:1024 sc1\n"
"v_dot2c_f32_bf16 v21, v159, v159\n"
"v_dot2c_f32_bf16 v20, v160, v160\n"
"v_dot2c_f32_bf16 v21, v161, v161\n"
"v_dot2c_f32_bf16 v20, v162, v162\n"
"v_dot2c_f32_bf16 v21, v163, v163\n"
"v_dot2c_f32_bf16 v20, v164, v164\n"
"v_dot2c_f32_bf16 v21, v165, v165\n"
"v_dot2c_f32_bf16 v20, v166, v166\n"
"v_dot2c_f32_bf16 v21, v167, v167\n"
"v_dot2c_f32_bf16 v20, v168, v168\n"
"v_dot2c_f32_bf16 v21, v169, v169\n"
"v_dot2c_f32_bf16 v20, v170, v170\n"
"v_dot2c_f32_bf16 v21, v171, v171\n"
"v_dot2c_f32_bf16 v20, v172, v172\n"
"v_dot2c_f32_bf16 v21, v173, v173\n"
"v_dot2c_f32_bf16 v20, v174, v174\n"
"v_dot2c_f32_bf16 v21, v175, v175\n"
"v_dot2c_f32_bf16 v20, v176, v176\n"
"v_dot2c_f32_bf16 v21, v177, v177\n"
"v_dot2c_f32_bf16 v20, v178, v178\n"
"buffer_load_dwordx4 a[48:51], v42, s[4:7], s75 offen offset:1024 sc1\n"
"v_dot2c_f32_bf16 v21, v179, v179\n"
"v_dot2c_f32_bf16 v20, v180, v180\n"
"v_dot2c_f32_bf16 v21, v181, v181\n"
"v_dot2c_f32_bf16 v20, v182, v182\n"
"v_dot2c_f32_bf16 v21, v183, v183\n"
"v_dot2c_f32_bf16 v20, v184, v184\n"
"v_dot2c_f32_bf16 v21, v185, v185\n"
"v_dot2c_f32_bf16 v20, v186, v186\n"
"v_dot2c_f32_bf16 v21, v187, v187\n"
"v_dot2c_f32_bf16 v20, v188, v188\n"
"v_dot2c_f32_bf16 v21, v189, v189\n"
"v_dot2c_f32_bf16 v20, v190, v190\n"
"v_dot2c_f32_bf16 v21, v191, v191\n"
"v_dot2c_f32_bf16 v20, v192, v192\n"
"v_dot2c_f32_bf16 v21, v193, v193\n"
"v_dot2c_f32_bf16 v20, v194, v194\n"
"v_dot2c_f32_bf16 v21, v195, v195\n"
"v_dot2c_f32_bf16 v20, v196, v196\n"
"v_dot2c_f32_bf16 v21, v197, v197\n"
"v_dot2c_f32_bf16 v20, v198, v198\n"
"buffer_load_dwordx4 a[52:55], v43, s[4:7], s75 offen offset:1024 sc1\n"
"v_dot2c_f32_bf16 v21, v199, v199\n"
"v_dot2c_f32_bf16 v20, v200, v200\n"
"v_dot2c_f32_bf16 v21, v201, v201\n"
"v_dot2c_f32_bf16 v20, v202, v202\n"
"v_dot2c_f32_bf16 v21, v203, v203\n"
"v_dot2c_f32_bf16 v20, v204, v204\n"
"v_dot2c_f32_bf16 v21, v205, v205\n"
"v_dot2c_f32_bf16 v20, v206, v206\n"
"v_dot2c_f32_bf16 v21, v207, v207\n"
"v_dot2c_f32_bf16 v20, v208, v208\n"
"v_dot2c_f32_bf16 v21, v209, v209\n"
"v_dot2c_f32_bf16 v20, v210, v210\n"
"v_dot2c_f32_bf16 v21, v211, v211\n"
"v_dot2c_f32_bf16 v20, v212, v212\n"
"v_dot2c_f32_bf16 v21, v213, v213\n"
"v_dot2c_f32_bf16 v20, v214, v214\n"
"v_dot2c_f32_bf16 v21, v215, v215\n"
"v_dot2c_f32_bf16 v20, v216, v216\n"
"v_dot2c_f32_bf16 v21, v217, v217\n"
"v_dot2c_f32_bf16 v20, v218, v218\n"
"buffer_load_dwordx4 a[56:59], v42, s[4:7], s77 offen offset:1024 sc1\n"
"v_dot2c_f32_bf16 v21, v219, v219\n"
"v_dot2c_f32_bf16 v20, v220, v220\n"
"v_dot2c_f32_bf16 v21, v221, v221\n"
"v_dot2c_f32_bf16 v20, v222, v222\n"
"v_dot2c_f32_bf16 v21, v223, v223\n"
"v_dot2c_f32_bf16 v20, v224, v224\n"
"v_dot2c_f32_bf16 v21, v225, v225\n"
"v_dot2c_f32_bf16 v20, v226, v226\n"
"v_dot2c_f32_bf16 v21, v227, v227\n"
"v_dot2c_f32_bf16 v20, v228, v228\n"
"v_dot2c_f32_bf16 v21, v229, v229\n"
"v_dot2c_f32_bf16 v20, v230, v230\n"
"v_dot2c_f32_bf16 v21, v231, v231\n"
"v_dot2c_f32_bf16 v20, v232, v232\n"
"v_dot2c_f32_bf16 v21, v233, v233\n"
"v_dot2c_f32_bf16 v20, v234, v234\n"
"v_dot2c_f32_bf16 v21, v235, v235\n"
"v_dot2c_f32_bf16 v20, v236, v236\n"
"v_dot2c_f32_bf16 v21, v237, v237\n"
"v_dot2c_f32_bf16 v20, v238, v238\n"
"buffer_load_dwordx4 a[60:63], v43, s[4:7], s77 offen offset:1024 sc1\n"
"v_dot2c_f32_bf16 v21, v239, v239\n"
"v_dot2c_f32_bf16 v20, v240, v240\n"
"v_dot2c_f32_bf16 v21, v241, v241\n"
"v_dot2c_f32_bf16 v20, v242, v242\n"
"v_dot2c_f32_bf16 v21, v243, v243\n"
"v_dot2c_f32_bf16 v20, v244, v244\n"
"v_dot2c_f32_bf16 v21, v245, v245\n"
"v_dot2c_f32_bf16 v20, v246, v246\n"
"v_dot2c_f32_bf16 v21, v247, v247\n"
"v_dot2c_f32_bf16 v20, v248, v248\n"
"v_dot2c_f32_bf16 v21, v249, v249\n"
"v_dot2c_f32_bf16 v20, v250, v250\n"
"v_dot2c_f32_bf16 v21, v251, v251\n"
"v_dot2c_f32_bf16 v20, v252, v252\n"
"v_dot2c_f32_bf16 v21, v253, v253\n"
"v_dot2c_f32_bf16 v20, v254, v254\n"
"v_dot2c_f32_bf16 v21, v255, v255\n"
"s_barrier\n"
"v_mfma_f32_32x32x16_bf16 v[112:127], v[64:67], v[128:131], 0\n"
"ds_read_b128 v[88:91], v2 offset:38912\n"
"v_accvgpr_read_b32 v18, a240\n"
"v_add_f32 v20, v20, v21\n"
"v_mul_f32 v18, 0xbb000000, v18\n"
"v_mov_b32 v21, v20\n"
"v_mul_f32 v19, v18, v18\n"
"v_mfma_f32_32x32x16_bf16 v[112:127], v[68:71], v[132:135], v[112:127]\n"
"ds_read_b128 v[92:95], v2 offset:39936\n"
"s_waitcnt lgkmcnt(4)\n"
"v_cvt_pk_bf16_f32 v53, v18, v18\n"
"v_lshlrev_b32 v53, 16, v53\n"
"v_permlane32_swap_b32 v20, v21\n"
"v_add_f32 v20, v20, v21\n"
"v_mfma_f32_32x32x16_bf16 v[112:127], v[72:75], v[136:139], v[112:127]\n"
"ds_read_b128 v[64:67], v2 offset:40960\n"
"buffer_store_dwordx4 v[32:35], v2, s[4:7], s67 offen offset:2048 sc1\n"
"v_mul_f32 v20, 0x3b000000, v20\n"
"v_sub_f32 v20, v20, v19\n"
"v_max_f32 v20, 0, v20\n"
"v_cvt_pk_bf16_f32 v20, v20, v20\n"
"v_mfma_f32_32x32x16_bf16 v[112:127], v[76:79], v[140:143], v[112:127]\n"
"ds_read_b128 v[68:71], v2 offset:41984\n"
"s_waitcnt lgkmcnt(4)\n"
"v_lshlrev_b32 v20, 16, v20\n"
"v_add_f32 v20, 0x3727c5ac, v20\n"
"v_cvt_pk_bf16_f32 v20, v20, v20\n"
"v_lshlrev_b32_e64 v20, 16, v20\n"
"v_mfma_f32_32x32x16_bf16 v[112:127], v[80:83], v[144:147], v[112:127]\n"
"ds_read_b128 v[72:75], v2 offset:43008\n"
"buffer_store_dwordx4 v[32:35], v2, s[4:7], s67 offen offset:3072 sc1\n"
"v_sqrt_f32 v20, v20\n"
"s_nop 0\n"
"v_cvt_pk_bf16_f32 v20, v20, v20\n"
"v_mfma_f32_32x32x16_bf16 v[112:127], v[84:87], v[148:151], v[112:127]\n"
"ds_read_b128 v[76:79], v2 offset:44032\n"
"s_waitcnt lgkmcnt(4)\n"
"v_lshlrev_b32 v20, 16, v20\n"
"v_rcp_f32 v21, v20\n"
"v_mul_f32 v55, v20, v20\n"
"v_mfma_f32_32x32x16_bf16 v[112:127], v[88:91], v[152:155], v[112:127]\n"
"ds_read_b128 v[80:83], v2 offset:45056\n"
"buffer_store_dwordx4 v[32:35], v2, s[4:7], s68 offen offset:0 sc1\n"
"v_mul_f32 v54, 0xbfb8aa3b, v21\n"
"v_fmac_f32_dpp v96, v56, v53 row_newbcast:0 row_mask:0xf bank_mask:0xf\n"
"v_fmac_f32_dpp v97, v56, v53 row_newbcast:1 row_mask:0xf bank_mask:0xf\n"
"v_fmac_f32_dpp v104, v56, v53 row_newbcast:8 row_mask:0xf bank_mask:0xf\n"
"v_mfma_f32_32x32x16_bf16 v[112:127], v[92:95], v[156:159], v[112:127]\n"
"ds_read_b128 v[84:87], v2 offset:46080\n"
"s_waitcnt lgkmcnt(4)\n"
"v_fmac_f32_dpp v105, v56, v53 row_newbcast:9 row_mask:0xf bank_mask:0xf\n"
"v_mul_f32 v22, v96, v54\n"
"v_mul_f32 v23, v97, v54\n"
"v_mfma_f32_32x32x16_bf16 v[112:127], v[64:67], v[160:163], v[112:127]\n"
"ds_read_b128 v[88:91], v2 offset:47104\n"
"v_exp_f32 v22, v22\n"
"v_exp_f32 v23, v23\n"
"v_mul_f32 v96, v96, v104\n"
"v_mfma_f32_32x32x16_bf16 v[112:127], v[68:71], v[164:167], v[112:127]\n"
"ds_read_b128 v[92:95], v2 offset:48128\n"
"s_waitcnt lgkmcnt(4)\n"
"buffer_store_dwordx4 v[32:35], v2, s[4:7], s68 offen offset:1024 sc1\n"
"v_mul_f32 v97, v97, v105\n"
"v_fma_f32 v22, v22, v55, v55\n"
"v_fma_f32 v23, v23, v55, v55\n"
"v_mfma_f32_32x32x16_bf16 v[112:127], v[72:75], v[168:171], v[112:127]\n"
"ds_read_b128 v[64:67], v2 offset:49152\n"
"v_rcp_f32 v22, v22\n"
"v_rcp_f32 v23, v23\n"
"v_mul_f32 v96, v96, v22\n"
"v_mfma_f32_32x32x16_bf16 v[112:127], v[76:79], v[172:175], v[112:127]\n"
"ds_read_b128 v[68:71], v2 offset:50176\n"
"s_waitcnt lgkmcnt(4)\n"
"buffer_store_dwordx4 v[32:35], v2, s[4:7], s68 offen offset:2048 sc1\n"
"v_mul_f32_e64 v97, v97, v23\n"
"v_cvt_pk_bf16_f32 v24, v96, v97\n"
"v_fmac_f32_dpp v98, v56, v53 row_newbcast:2 row_mask:0xf bank_mask:0xf\n"
"v_mfma_f32_32x32x16_bf16 v[112:127], v[80:83], v[176:179], v[112:127]\n"
"ds_read_b128 v[72:75], v2 offset:51200\n"
"v_fmac_f32_dpp v99, v56, v53 row_newbcast:3 row_mask:0xf bank_mask:0xf\n"
"v_fmac_f32_dpp v106, v56, v53 row_newbcast:10 row_mask:0xf bank_mask:0xf\n"
"v_fmac_f32_dpp v107, v56, v53 row_newbcast:11 row_mask:0xf bank_mask:0xf\n"
"v_mul_f32 v22, v98, v54\n"
"v_mul_f32 v23, v99, v54\n"
"v_mfma_f32_32x32x16_bf16 v[112:127], v[84:87], v[180:183], v[112:127]\n"
"ds_read_b128 v[76:79], v2 offset:52224\n"
"s_waitcnt lgkmcnt(4)\n"
"buffer_load_dwordx4 a[64:67], v42, s[4:7], s71 offen offset:2048 sc1\n"
"v_exp_f32 v22, v22\n"
"v_mfma_f32_32x32x16_bf16 v[112:127], v[88:91], v[184:187], v[112:127]\n"
"ds_read_b128 v[80:83], v2 offset:53248\n"
"v_exp_f32 v23, v23\n"
"v_mul_f32 v98, v98, v106\n"
"v_mul_f32_e64 v99, v99, v107\n"
"v_fma_f32 v22, v22, v55, v55\n"
"v_mfma_f32_32x32x16_bf16 v[112:127], v[92:95], v[188:191], v[112:127]\n"
"ds_read_b128 v[84:87], v2 offset:54272\n"
"s_waitcnt lgkmcnt(4)\n"
"v_fma_f32 v23, v23, v55, v55\n"
"v_rcp_f32 v22, v22\n"
"v_mfma_f32_32x32x16_bf16 v[112:127], v[64:67], v[192:195], v[112:127]\n"
"ds_read_b128 v[88:91], v2 offset:55296\n"
"buffer_load_dwordx4 a[68:71], v43, s[4:7], s71 offen offset:2048 sc1\n"
"v_rcp_f32 v23, v23\n"
"v_mul_f32 v98, v98, v22\n"
"v_mul_f32_e64 v99, v99, v23\n"
"v_mfma_f32_32x32x16_bf16 v[112:127], v[68:71], v[196:199], v[112:127]\n"
"ds_read_b128 v[92:95], v2 offset:56320\n"
"s_waitcnt lgkmcnt(4)\n"
"v_cvt_pk_bf16_f32 v25, v98, v99\n"
"s_nop 0\n"
"v_fmac_f32_dpp v100, v56, v53 row_newbcast:4 row_mask:0xf bank_mask:0xf\n"
"v_fmac_f32_dpp v101, v56, v53 row_newbcast:5 row_mask:0xf bank_mask:0xf\n"
"v_fmac_f32_dpp v108, v56, v53 row_newbcast:12 row_mask:0xf bank_mask:0xf\n"
"v_mfma_f32_32x32x16_bf16 v[112:127], v[72:75], v[200:203], v[112:127]\n"
"ds_read_b128 v[64:67], v2 offset:57344\n"
"buffer_load_dwordx4 a[72:75], v42, s[4:7], s73 offen offset:2048 sc1\n"
"v_fmac_f32_dpp v109, v56, v53 row_newbcast:13 row_mask:0xf bank_mask:0xf\n"
"v_mul_f32 v22, v100, v54\n"
"v_mul_f32 v23, v101, v54\n"
"v_mfma_f32_32x32x16_bf16 v[112:127], v[76:79], v[204:207], v[112:127]\n"
"ds_read_b128 v[68:71], v2 offset:58368\n"
"s_waitcnt lgkmcnt(4)\n"
"v_exp_f32 v22, v22\n"
"v_exp_f32_e64 v23, v23\n"
"v_mfma_f32_32x32x16_bf16 v[112:127], v[80:83], v[208:211], v[112:127]\n"
"ds_read_b128 v[72:75], v2 offset:59392\n"
"buffer_load_dwordx4 a[76:79], v43, s[4:7], s73 offen offset:2048 sc1\n"
"v_mul_f32 v100, v100, v108\n"
"v_mul_f32 v101, v101, v109\n"
"v_fma_f32 v22, v22, v55, v55\n"
"v_fma_f32 v23, v23, v55, v55\n"
"v_mfma_f32_32x32x16_bf16 v[112:127], v[84:87], v[212:215], v[112:127]\n"
"ds_read_b128 v[76:79], v2 offset:60416\n"
"s_waitcnt lgkmcnt(4)\n"
"v_rcp_f32 v22, v22\n"
"v_rcp_f32 v23, v23\n"
"v_mfma_f32_32x32x16_bf16 v[112:127], v[88:91], v[216:219], v[112:127]\n"
"ds_read_b128 v[80:83], v2 offset:61440\n"
"v_mul_f32 v100, v100, v22\n"
"v_mul_f32_e64 v101, v101, v23\n"
"v_cvt_pk_bf16_f32 v26, v100, v101\n"
"v_fmac_f32_dpp v102, v56, v53 row_newbcast:6 row_mask:0xf bank_mask:0xf\n"
"v_fmac_f32_dpp v103, v56, v53 row_newbcast:7 row_mask:0xf bank_mask:0xf\n"
"v_mfma_f32_32x32x16_bf16 v[112:127], v[92:95], v[220:223], v[112:127]\n"
"ds_read_b128 v[84:87], v2 offset:62464\n"
"s_waitcnt lgkmcnt(4)\n"
"buffer_load_dwordx4 a[80:83], v42, s[4:7], s75 offen offset:2048 sc1\n"
"v_fmac_f32_dpp v110, v56, v53 row_newbcast:14 row_mask:0xf bank_mask:0xf\n"
"v_fmac_f32_dpp v111, v56, v53 row_newbcast:15 row_mask:0xf bank_mask:0xf\n"
"v_mul_f32 v22, v102, v54\n"
"v_mfma_f32_32x32x16_bf16 v[112:127], v[64:67], v[224:227], v[112:127]\n"
"ds_read_b128 v[88:91], v2 offset:63488\n"
"v_mul_f32 v23, v103, v54\n"
"v_exp_f32 v22, v22\n"
"v_exp_f32 v23, v23\n"
"v_mfma_f32_32x32x16_bf16 v[112:127], v[68:71], v[228:231], v[112:127]\n"
"ds_read_b128 v[92:95], v2 offset:64512\n"
"s_waitcnt lgkmcnt(4)\n"
"buffer_load_dwordx4 a[84:87], v43, s[4:7], s75 offen offset:2048 sc1\n"
"v_mul_f32 v102, v102, v110\n"
"v_mul_f32 v103, v103, v111\n"
"v_fma_f32 v22, v22, v55, v55\n"
"v_mfma_f32_32x32x16_bf16 v[112:127], v[72:75], v[232:235], v[112:127]\n"
"ds_read_b128 v[64:67], v47 offset:0\n"
"v_fma_f32 v23, v23, v55, v55\n"
"v_rcp_f32 v22, v22\n"
"v_rcp_f32 v23, v23\n"
"v_mfma_f32_32x32x16_bf16 v[112:127], v[76:79], v[236:239], v[112:127]\n"
"ds_read_b128 v[68:71], v47 offset:1024\n"
"s_waitcnt lgkmcnt(4)\n"
"buffer_load_dwordx4 a[88:91], v42, s[4:7], s77 offen offset:2048 sc1\n"
"v_mul_f32 v102, v102, v22\n"
"v_mul_f32_e64 v103, v103, v23\n"
"v_cvt_pk_bf16_f32 v27, v102, v103\n"
"v_mfma_f32_32x32x16_bf16 v[112:127], v[80:83], v[240:243], v[112:127]\n"
"ds_read_b128 v[72:75], v47 offset:2048\n"
"buffer_store_dwordx4 v[24:27], v2, s[4:7], s67 offen offset:0 sc1\n"
"v_mfma_f32_32x32x16_bf16 v[112:127], v[84:87], v[244:247], v[112:127]\n"
"ds_read_b128 v[76:79], v47 offset:3072\n"
"s_waitcnt lgkmcnt(4)\n"
"v_mfma_f32_32x32x16_bf16 v[112:127], v[88:91], v[248:251], v[112:127]\n"
"ds_read_b128 v[80:83], v47 offset:4096\n"
"buffer_load_dwordx4 a[92:95], v43, s[4:7], s77 offen offset:2048 sc1\n"
"v_mfma_f32_32x32x16_bf16 v[112:127], v[92:95], v[252:255], v[112:127]\n"
"ds_read_b128 v[84:87], v47 offset:5120\n"
"s_waitcnt lgkmcnt(4)\n"
"s_barrier\n"
"v_mfma_f32_32x32x16_bf16 v[96:111], v[64:67], v[128:131], 0\n"
"ds_read_b128 v[88:91], v47 offset:6144\n"
"s_add_u32 m0, s70, 0\n"
"s_add_u32 s35, s20, 163840\n"
"buffer_load_dwordx4 v2, s[4:7], s35 offen sc1 lds\n"
"v_mfma_f32_32x32x16_bf16 v[96:111], v[68:71], v[132:135], v[96:111]\n"
"ds_read_b128 v[92:95], v47 offset:7168\n"
"s_waitcnt lgkmcnt(4)\n"
"s_nop 0\n"
"v_mfma_f32_32x32x16_bf16 v[96:111], v[72:75], v[136:139], v[96:111]\n"
"ds_read_b128 v[64:67], v47 offset:8192\n"
"buffer_load_dwordx4 v2, s[4:7], s35 offen offset:1024 sc1 lds\n"
"v_mfma_f32_32x32x16_bf16 v[96:111], v[76:79], v[140:143], v[96:111]\n"
"ds_read_b128 v[68:71], v47 offset:9216\n"
"s_waitcnt lgkmcnt(4)\n"
"v_fmac_f32_dpp v112, v57, v53 row_newbcast:0 row_mask:0xf bank_mask:0xf\n"
"v_fmac_f32_dpp v113, v57, v53 row_newbcast:1 row_mask:0xf bank_mask:0xf\n"
"v_fmac_f32_dpp v120, v57, v53 row_newbcast:8 row_mask:0xf bank_mask:0xf\n"
"v_fmac_f32_dpp v121, v57, v53 row_newbcast:9 row_mask:0xf bank_mask:0xf\n"
"v_mfma_f32_32x32x16_bf16 v[96:111], v[80:83], v[144:147], v[96:111]\n"
"ds_read_b128 v[72:75], v47 offset:10240\n"
"v_mul_f32 v22, v112, v54\n"
"v_mul_f32 v23, v113, v54\n"
"v_exp_f32_e64 v22, v22\n"
"v_mfma_f32_32x32x16_bf16 v[96:111], v[84:87], v[148:151], v[96:111]\n"
"ds_read_b128 v[76:79], v47 offset:11264\n"
"s_waitcnt lgkmcnt(4)\n"
"buffer_load_dwordx4 v2, s[4:7], s35 offen offset:2048 sc1 lds\n"
"v_exp_f32 v23, v23\n"
"v_mul_f32 v112, v112, v120\n"
"v_mfma_f32_32x32x16_bf16 v[96:111], v[88:91], v[152:155], v[96:111]\n"
"ds_read_b128 v[80:83], v47 offset:12288\n"
"v_mul_f32 v113, v113, v121\n"
"v_fma_f32 v22, v22, v55, v55\n"
"v_fma_f32 v23, v23, v55, v55\n"
"v_rcp_f32 v22, v22\n"
"v_mfma_f32_32x32x16_bf16 v[96:111], v[92:95], v[156:159], v[96:111]\n"
"ds_read_b128 v[84:87], v47 offset:13312\n"
"s_waitcnt lgkmcnt(4)\n"
"buffer_load_dwordx4 v2, s[4:7], s35 offen offset:3072 sc1 lds\n"
"v_rcp_f32 v23, v23\n"
"v_mul_f32_e64 v112, v112, v22\n"
"v_mfma_f32_32x32x16_bf16 v[96:111], v[64:67], v[160:163], v[96:111]\n"
"ds_read_b128 v[88:91], v47 offset:14336\n"
"v_mul_f32_e64 v113, v113, v23\n"
"v_cvt_pk_bf16_f32 v28, v112, v113\n"
"v_fmac_f32_dpp v114, v57, v53 row_newbcast:2 row_mask:0xf bank_mask:0xf\n"
"v_fmac_f32_dpp v115, v57, v53 row_newbcast:3 row_mask:0xf bank_mask:0xf\n"
"v_fmac_f32_dpp v122, v57, v53 row_newbcast:10 row_mask:0xf bank_mask:0xf\n"
"v_mfma_f32_32x32x16_bf16 v[96:111], v[68:71], v[164:167], v[96:111]\n"
"ds_read_b128 v[92:95], v47 offset:15360\n"
"s_waitcnt lgkmcnt(4)\n"
"s_add_u32 m0, s70, 4096\n"
"s_add_u32 s35, s21, 163840\n"
"buffer_load_dwordx4 v2, s[4:7], s35 offen sc1 lds\n"
"v_fmac_f32_dpp v123, v57, v53 row_newbcast:11 row_mask:0xf bank_mask:0xf\n"
"v_mfma_f32_32x32x16_bf16 v[96:111], v[72:75], v[168:171], v[96:111]\n"
"ds_read_b128 v[64:67], v47 offset:16384\n"
"v_mul_f32 v22, v114, v54\n"
"v_mul_f32 v23, v115, v54\n"
"v_exp_f32 v22, v22\n"
"v_mfma_f32_32x32x16_bf16 v[96:111], v[76:79], v[172:175], v[96:111]\n"
"ds_read_b128 v[68:71], v47 offset:17408\n"
"s_waitcnt lgkmcnt(4)\n"
"v_exp_f32 v23, v23\n"
"v_mul_f32 v114, v114, v122\n"
"v_mul_f32 v115, v115, v123\n"
"v_mfma_f32_32x32x16_bf16 v[96:111], v[80:83], v[176:179], v[96:111]\n"
"ds_read_b128 v[72:75], v47 offset:18432\n"
"buffer_load_dwordx4 v2, s[4:7], s35 offen offset:1024 sc1 lds\n"
"v_fma_f32 v22, v22, v55, v55\n"
"v_fma_f32 v23, v23, v55, v55\n"
"v_rcp_f32_e64 v22, v22\n"
"v_mfma_f32_32x32x16_bf16 v[96:111], v[84:87], v[180:183], v[96:111]\n"
"ds_read_b128 v[76:79], v47 offset:19456\n"
"s_waitcnt lgkmcnt(4)\n"
"v_rcp_f32 v23, v23\n"
"v_mul_f32 v114, v114, v22\n"
"v_mul_f32 v115, v115, v23\n"
"v_mfma_f32_32x32x16_bf16 v[96:111], v[88:91], v[184:187], v[96:111]\n"
"ds_read_b128 v[80:83], v47 offset:20480\n"
"buffer_load_dwordx4 v2, s[4:7], s35 offen offset:2048 sc1 lds\n"
"v_cvt_pk_bf16_f32 v29, v114, v115\n"
"v_fmac_f32_dpp v116, v57, v53 row_newbcast:4 row_mask:0xf bank_mask:0xf\n"
"v_fmac_f32_dpp v117, v57, v53 row_newbcast:5 row_mask:0xf bank_mask:0xf\n"
"v_fmac_f32_dpp v124, v57, v53 row_newbcast:12 row_mask:0xf bank_mask:0xf\n"
"v_mfma_f32_32x32x16_bf16 v[96:111], v[92:95], v[188:191], v[96:111]\n"
"ds_read_b128 v[84:87], v47 offset:21504\n"
"s_waitcnt lgkmcnt(4)\n"
"v_fmac_f32_dpp v125, v57, v53 row_newbcast:13 row_mask:0xf bank_mask:0xf\n"
"v_mul_f32 v22, v116, v54\n"
"v_mul_f32_e64 v23, v117, v54\n"
"v_mfma_f32_32x32x16_bf16 v[96:111], v[64:67], v[192:195], v[96:111]\n"
"ds_read_b128 v[88:91], v47 offset:22528\n"
"buffer_load_dwordx4 v2, s[4:7], s35 offen offset:3072 sc1 lds\n"
"v_exp_f32 v22, v22\n"
"v_exp_f32 v23, v23\n"
"v_mfma_f32_32x32x16_bf16 v[96:111], v[68:71], v[196:199], v[96:111]\n"
"ds_read_b128 v[92:95], v47 offset:23552\n"
"s_waitcnt lgkmcnt(4)\n"
"v_mul_f32 v116, v116, v124\n"
"v_mul_f32_e64 v117, v117, v125\n"
"v_fma_f32 v22, v22, v55, v55\n"
"v_fma_f32 v23, v23, v55, v55\n"
"v_mfma_f32_32x32x16_bf16 v[96:111], v[72:75], v[200:203], v[96:111]\n"
"ds_read_b128 v[64:67], v47 offset:24576\n"
"v_rcp_f32 v22, v22\n"
"v_rcp_f32 v23, v23\n"
"v_mul_f32 v116, v116, v22\n"
"v_mfma_f32_32x32x16_bf16 v[96:111], v[76:79], v[204:207], v[96:111]\n"
"ds_read_b128 v[68:71], v47 offset:25600\n"
"s_waitcnt lgkmcnt(4)\n"
"s_add_u32 m0, s70, 32768\n"
"s_add_u32 s35, s20, 196608\n"
"buffer_load_dwordx4 v2, s[4:7], s35 offen sc1 lds\n"
"v_mul_f32_e64 v117, v117, v23\n"
"v_mfma_f32_32x32x16_bf16 v[96:111], v[80:83], v[208:211], v[96:111]\n"
"ds_read_b128 v[72:75], v47 offset:26624\n"
"v_cvt_pk_bf16_f32 v30, v116, v117\n"
"v_fmac_f32_dpp v118, v57, v53 row_newbcast:6 row_mask:0xf bank_mask:0xf\n"
"v_fmac_f32_dpp v119, v57, v53 row_newbcast:7 row_mask:0xf bank_mask:0xf\n"
"v_fmac_f32_dpp v126, v57, v53 row_newbcast:14 row_mask:0xf bank_mask:0xf\n"
"v_fmac_f32_dpp v127, v57, v53 row_newbcast:15 row_mask:0xf bank_mask:0xf\n"
"v_mfma_f32_32x32x16_bf16 v[96:111], v[84:87], v[212:215], v[96:111]\n"
"ds_read_b128 v[76:79], v47 offset:27648\n"
"s_waitcnt lgkmcnt(4)\n"
"buffer_load_dwordx4 v2, s[4:7], s35 offen offset:1024 sc1 lds\n"
"v_mul_f32 v22, v118, v54\n"
"v_mul_f32_e64 v23, v119, v54\n"
"v_mfma_f32_32x32x16_bf16 v[96:111], v[88:91], v[216:219], v[96:111]\n"
"ds_read_b128 v[80:83], v47 offset:28672\n"
"v_exp_f32 v22, v22\n"
"v_exp_f32 v23, v23\n"
"v_mul_f32_e64 v118, v118, v126\n"
"v_mfma_f32_32x32x16_bf16 v[96:111], v[92:95], v[220:223], v[96:111]\n"
"ds_read_b128 v[84:87], v47 offset:29696\n"
"s_waitcnt lgkmcnt(4)\n"
"buffer_load_dwordx4 v2, s[4:7], s35 offen offset:2048 sc1 lds\n"
"v_mul_f32 v119, v119, v127\n"
"v_fma_f32 v22, v22, v55, v55\n"
"v_fma_f32 v23, v23, v55, v55\n"
"v_mfma_f32_32x32x16_bf16 v[96:111], v[64:67], v[224:227], v[96:111]\n"
"ds_read_b128 v[88:91], v47 offset:30720\n"
"v_rcp_f32 v22, v22\n"
"v_rcp_f32 v23, v23\n"
"v_mul_f32 v118, v118, v22\n"
"v_mfma_f32_32x32x16_bf16 v[96:111], v[68:71], v[228:231], v[96:111]\n"
"ds_read_b128 v[92:95], v47 offset:31744\n"
"s_waitcnt lgkmcnt(4)\n"
"v_mul_f32_e64 v119, v119, v23\n"
"v_cvt_pk_bf16_f32 v31, v118, v119\n"
"buffer_store_dwordx4 v[28:31], v2, s[4:7], s67 offen offset:1024 sc1\n"
"v_mfma_f32_32x32x16_bf16 v[96:111], v[72:75], v[232:235], v[96:111]\n"
"s_waitcnt vmcnt(63)\n"
"s_barrier\n"
"ds_read_b128 v[64:67], v47 offset:32768\n"
"buffer_load_dwordx4 v2, s[4:7], s35 offen offset:3072 sc1 lds\n"
"v_mfma_f32_32x32x16_bf16 v[96:111], v[76:79], v[236:239], v[96:111]\n"
"ds_read_b128 v[68:71], v47 offset:33792\n"
"s_waitcnt lgkmcnt(4)\n"
"v_mfma_f32_32x32x16_bf16 v[96:111], v[80:83], v[240:243], v[96:111]\n"
"ds_read_b128 v[72:75], v47 offset:34816\n"
"s_add_u32 m0, s70, 36864\n"
"s_add_u32 s35, s21, 196608\n"
"buffer_load_dwordx4 v2, s[4:7], s35 offen sc1 lds\n"
"v_mfma_f32_32x32x16_bf16 v[96:111], v[84:87], v[244:247], v[96:111]\n"
"ds_read_b128 v[76:79], v47 offset:35840\n"
"s_waitcnt lgkmcnt(4)\n"
"v_mfma_f32_32x32x16_bf16 v[96:111], v[88:91], v[248:251], v[96:111]\n"
"ds_read_b128 v[80:83], v47 offset:36864\n"
"buffer_load_dwordx4 v2, s[4:7], s35 offen offset:1024 sc1 lds\n"
"v_mfma_f32_32x32x16_bf16 v[96:111], v[92:95], v[252:255], v[96:111]\n"
"ds_read_b128 v[84:87], v47 offset:37888\n"
"s_waitcnt lgkmcnt(4)\n"
"v_mfma_f32_32x32x16_bf16 v[112:127], v[64:67], v[128:131], 0\n"
"ds_read_b128 v[88:91], v47 offset:38912\n"
"v_mfma_f32_32x32x16_bf16 v[112:127], v[68:71], v[132:135], v[112:127]\n"
"ds_read_b128 v[92:95], v47 offset:39936\n"
"s_waitcnt lgkmcnt(4)\n"
"buffer_load_dwordx4 v2, s[4:7], s35 offen offset:2048 sc1 lds\n"
"v_mfma_f32_32x32x16_bf16 v[112:127], v[72:75], v[136:139], v[112:127]\n"
"ds_read_b128 v[64:67], v47 offset:40960\n"
"v_mfma_f32_32x32x16_bf16 v[112:127], v[76:79], v[140:143], v[112:127]\n"
"ds_read_b128 v[68:71], v47 offset:41984\n"
"s_waitcnt lgkmcnt(4)\n"
"s_nop 0\n"
"buffer_load_dwordx4 v2, s[4:7], s35 offen offset:3072 sc1 lds\n"
"v_fmac_f32_dpp v96, v58, v53 row_newbcast:0 row_mask:0xf bank_mask:0xf\n"
"v_fmac_f32_dpp v97, v58, v53 row_newbcast:1 row_mask:0xf bank_mask:0xf\n"
"v_fmac_f32_dpp v104, v58, v53 row_newbcast:8 row_mask:0xf bank_mask:0xf\n"
"v_mfma_f32_32x32x16_bf16 v[112:127], v[80:83], v[144:147], v[112:127]\n"
"ds_read_b128 v[72:75], v47 offset:43008\n"
"v_fmac_f32_dpp v105, v58, v53 row_newbcast:9 row_mask:0xf bank_mask:0xf\n"
"v_mul_f32 v22, v96, v54\n"
"v_mul_f32 v23, v97, v54\n"
"v_exp_f32 v22, v22\n"
"v_mfma_f32_32x32x16_bf16 v[112:127], v[84:87], v[148:151], v[112:127]\n"
"ds_read_b128 v[76:79], v47 offset:44032\n"
"s_waitcnt lgkmcnt(4)\n"
"buffer_load_dwordx4 a[96:99], v42, s[4:7], s71 offen offset:3072 sc1\n"
"v_exp_f32 v23, v23\n"
"v_mul_f32 v96, v96, v104\n"
"v_mfma_f32_32x32x16_bf16 v[112:127], v[88:91], v[152:155], v[112:127]\n"
"ds_read_b128 v[80:83], v47 offset:45056\n"
"v_mul_f32_e64 v97, v97, v105\n"
"v_fma_f32 v22, v22, v55, v55\n"
"v_fma_f32 v23, v23, v55, v55\n"
"v_rcp_f32_e64 v22, v22\n"
"v_mfma_f32_32x32x16_bf16 v[112:127], v[92:95], v[156:159], v[112:127]\n"
"ds_read_b128 v[84:87], v47 offset:46080\n"
"s_waitcnt lgkmcnt(4)\n"
"v_rcp_f32 v23, v23\n"
"v_mul_f32 v96, v96, v22\n"
"v_mul_f32 v97, v97, v23\n"
"v_mfma_f32_32x32x16_bf16 v[112:127], v[64:67], v[160:163], v[112:127]\n"
"ds_read_b128 v[88:91], v47 offset:47104\n"
"buffer_load_dwordx4 a[100:103], v43, s[4:7], s71 offen offset:3072 sc1\n"
"v_cvt_pk_bf16_f32 v24, v96, v97\n"
"v_fmac_f32_dpp v98, v58, v53 row_newbcast:2 row_mask:0xf bank_mask:0xf\n"
"v_fmac_f32_dpp v99, v58, v53 row_newbcast:3 row_mask:0xf bank_mask:0xf\n"
"v_fmac_f32_dpp v106, v58, v53 row_newbcast:10 row_mask:0xf bank_mask:0xf\n"
"v_mfma_f32_32x32x16_bf16 v[112:127], v[68:71], v[164:167], v[112:127]\n"
"ds_read_b128 v[92:95], v47 offset:48128\n"
"s_waitcnt lgkmcnt(4)\n"
"v_fmac_f32_dpp v107, v58, v53 row_newbcast:11 row_mask:0xf bank_mask:0xf\n"
"v_mul_f32 v22, v98, v54\n"
"v_mul_f32_e64 v23, v99, v54\n"
"v_mfma_f32_32x32x16_bf16 v[112:127], v[72:75], v[168:171], v[112:127]\n"
"ds_read_b128 v[64:67], v47 offset:49152\n"
"buffer_load_dwordx4 a[104:107], v42, s[4:7], s73 offen offset:3072 sc1\n"
"v_exp_f32 v22, v22\n"
"v_exp_f32 v23, v23\n"
"v_mfma_f32_32x32x16_bf16 v[112:127], v[76:79], v[172:175], v[112:127]\n"
"ds_read_b128 v[68:71], v47 offset:50176\n"
"s_waitcnt lgkmcnt(4)\n"
"v_mul_f32 v98, v98, v106\n"
"v_mul_f32_e64 v99, v99, v107\n"
"v_fma_f32 v22, v22, v55, v55\n"
"v_fma_f32 v23, v23, v55, v55\n"
"v_mfma_f32_32x32x16_bf16 v[112:127], v[80:83], v[176:179], v[112:127]\n"
"ds_read_b128 v[72:75], v47 offset:51200\n"
"buffer_load_dwordx4 a[108:111], v43, s[4:7], s73 offen offset:3072 sc1\n"
"v_rcp_f32 v22, v22\n"
"v_rcp_f32 v23, v23\n"
"v_mfma_f32_32x32x16_bf16 v[112:127], v[84:87], v[180:183], v[112:127]\n"
"ds_read_b128 v[76:79], v47 offset:52224\n"
"s_waitcnt lgkmcnt(4)\n"
"v_mul_f32 v98, v98, v22\n"
"v_mul_f32_e64 v99, v99, v23\n"
"v_cvt_pk_bf16_f32 v25, v98, v99\n"
"v_fmac_f32_dpp v100, v58, v53 row_newbcast:4 row_mask:0xf bank_mask:0xf\n"
"v_mfma_f32_32x32x16_bf16 v[112:127], v[88:91], v[184:187], v[112:127]\n"
"ds_read_b128 v[80:83], v47 offset:53248\n"
"s_waitcnt vmcnt(9)\n"
"buffer_store_dwordx4 v[36:39], v5, s[4:7], s79 offen offset:128 sc1\n"
"v_fmac_f32_dpp v101, v58, v53 row_newbcast:5 row_mask:0xf bank_mask:0xf\n"
"v_fmac_f32_dpp v108, v58, v53 row_newbcast:12 row_mask:0xf bank_mask:0xf\n"
"v_fmac_f32_dpp v109, v58, v53 row_newbcast:13 row_mask:0xf bank_mask:0xf\n"
"v_mfma_f32_32x32x16_bf16 v[112:127], v[92:95], v[188:191], v[112:127]\n"
"ds_read_b128 v[84:87], v47 offset:54272\n"
"s_waitcnt lgkmcnt(4)\n"
"buffer_load_dwordx4 a[112:115], v42, s[4:7], s75 offen offset:3072 sc1\n"
"v_mul_f32 v22, v100, v54\n"
"v_mul_f32 v23, v101, v54\n"
"v_mfma_f32_32x32x16_bf16 v[112:127], v[64:67], v[192:195], v[112:127]\n"
"ds_read_b128 v[88:91], v47 offset:55296\n"
"v_exp_f32 v22, v22\n"
"v_exp_f32 v23, v23\n"
"v_mul_f32 v100, v100, v108\n"
"v_mfma_f32_32x32x16_bf16 v[112:127], v[68:71], v[196:199], v[112:127]\n"
"ds_read_b128 v[92:95], v47 offset:56320\n"
"s_waitcnt lgkmcnt(4)\n"
"buffer_load_dwordx4 a[116:119], v43, s[4:7], s75 offen offset:3072 sc1\n"
"v_mul_f32_e64 v101, v101, v109\n"
"v_fma_f32 v22, v22, v55, v55\n"
"v_fma_f32 v23, v23, v55, v55\n"
"v_mfma_f32_32x32x16_bf16 v[112:127], v[72:75], v[200:203], v[112:127]\n"
"ds_read_b128 v[64:67], v47 offset:57344\n"
"v_rcp_f32 v22, v22\n"
"v_rcp_f32 v23, v23\n"
"v_mul_f32_e64 v100, v100, v22\n"
"v_mfma_f32_32x32x16_bf16 v[112:127], v[76:79], v[204:207], v[112:127]\n"
"ds_read_b128 v[68:71], v47 offset:58368\n"
"s_waitcnt lgkmcnt(4)\n"
"buffer_load_dwordx4 a[120:123], v42, s[4:7], s77 offen offset:3072 sc1\n"
"v_mul_f32 v101, v101, v23\n"
"v_cvt_pk_bf16_f32 v26, v100, v101\n"
"v_fmac_f32_dpp v102, v58, v53 row_newbcast:6 row_mask:0xf bank_mask:0xf\n"
"v_mfma_f32_32x32x16_bf16 v[112:127], v[80:83], v[208:211], v[112:127]\n"
"ds_read_b128 v[72:75], v47 offset:59392\n"
"v_fmac_f32_dpp v103, v58, v53 row_newbcast:7 row_mask:0xf bank_mask:0xf\n"
"v_fmac_f32_dpp v110, v58, v53 row_newbcast:14 row_mask:0xf bank_mask:0xf\n"
"v_fmac_f32_dpp v111, v58, v53 row_newbcast:15 row_mask:0xf bank_mask:0xf\n"
"v_mul_f32 v22, v102, v54\n"
"v_mul_f32 v23, v103, v54\n"
"v_mfma_f32_32x32x16_bf16 v[112:127], v[84:87], v[212:215], v[112:127]\n"
"ds_read_b128 v[76:79], v47 offset:60416\n"
"s_waitcnt lgkmcnt(4)\n"
"v_exp_f32 v22, v22\n"
"v_exp_f32_e64 v23, v23\n"
"v_mfma_f32_32x32x16_bf16 v[112:127], v[88:91], v[216:219], v[112:127]\n"
"ds_read_b128 v[80:83], v47 offset:61440\n"
"buffer_load_dwordx4 a[124:127], v43, s[4:7], s77 offen offset:3072 sc1\n"
"v_mul_f32 v102, v102, v110\n"
"v_mul_f32 v103, v103, v111\n"
"v_fma_f32 v22, v22, v55, v55\n"
"v_fma_f32 v23, v23, v55, v55\n"
"v_mfma_f32_32x32x16_bf16 v[112:127], v[92:95], v[220:223], v[112:127]\n"
"ds_read_b128 v[84:87], v47 offset:62464\n"
"s_waitcnt lgkmcnt(4)\n"
"v_rcp_f32 v22, v22\n"
"v_rcp_f32_e64 v23, v23\n"
"v_mfma_f32_32x32x16_bf16 v[112:127], v[64:67], v[224:227], v[112:127]\n"
"ds_read_b128 v[88:91], v47 offset:63488\n"
"buffer_load_dwordx4 a[128:131], v42, s[4:7], s72 offen offset:0 sc1\n"
"v_mul_f32 v102, v102, v22\n"
"v_mul_f32 v103, v103, v23\n"
"v_cvt_pk_bf16_f32 v27, v102, v103\n"
"buffer_store_dwordx4 v[24:27], v2, s[4:7], s67 offen offset:2048 sc1\n"
"v_mfma_f32_32x32x16_bf16 v[112:127], v[68:71], v[228:231], v[112:127]\n"
"ds_read_b128 v[92:95], v47 offset:64512\n"
"s_waitcnt lgkmcnt(4)\n"
"v_mfma_f32_32x32x16_bf16 v[112:127], v[72:75], v[232:235], v[112:127]\n"
"ds_read_b128 v[64:67], v50 offset:0\n"
"buffer_load_dwordx4 a[132:135], v43, s[4:7], s72 offen offset:0 sc1\n"
"v_mfma_f32_32x32x16_bf16 v[112:127], v[76:79], v[236:239], v[112:127]\n"
"ds_read_b128 v[68:71], v50 offset:1024\n"
"s_waitcnt lgkmcnt(4)\n"
"v_mfma_f32_32x32x16_bf16 v[112:127], v[80:83], v[240:243], v[112:127]\n"
"ds_read_b128 v[72:75], v50 offset:2048\n"
"v_mfma_f32_32x32x16_bf16 v[112:127], v[84:87], v[244:247], v[112:127]\n"
"ds_read_b128 v[76:79], v50 offset:3072\n"
"s_waitcnt lgkmcnt(4)\n"
"buffer_load_dwordx4 a[136:139], v42, s[4:7], s74 offen offset:0 sc1\n"
"v_mfma_f32_32x32x16_bf16 v[112:127], v[88:91], v[248:251], v[112:127]\n"
"ds_read_b128 v[80:83], v50 offset:4096\n"
"v_mfma_f32_32x32x16_bf16 v[112:127], v[92:95], v[252:255], v[112:127]\n"
"ds_read_b128 v[84:87], v50 offset:5120\n"
"s_waitcnt lgkmcnt(4)\n"
"buffer_load_dwordx4 a[140:143], v43, s[4:7], s74 offen offset:0 sc1\n"
"v_mfma_f32_32x32x16_bf16 v[96:111], v[64:67], v[128:131], 0\n"
"ds_read_b128 v[88:91], v50 offset:6144\n"
"v_mfma_f32_32x32x16_bf16 v[96:111], v[68:71], v[132:135], v[96:111]\n"
"ds_read_b128 v[92:95], v50 offset:7168\n"
"s_waitcnt lgkmcnt(4)\n"
"s_nop 0\n"
"buffer_load_dwordx4 a[144:147], v42, s[4:7], s76 offen offset:0 sc1\n"
"v_mfma_f32_32x32x16_bf16 v[96:111], v[72:75], v[136:139], v[96:111]\n"
"ds_read_b128 v[64:67], v50 offset:8192\n"
"v_mfma_f32_32x32x16_bf16 v[96:111], v[76:79], v[140:143], v[96:111]\n"
"ds_read_b128 v[68:71], v50 offset:9216\n"
"s_waitcnt lgkmcnt(4)\n"
"v_fmac_f32_dpp v112, v59, v53 row_newbcast:0 row_mask:0xf bank_mask:0xf\n"
"v_fmac_f32_dpp v113, v59, v53 row_newbcast:1 row_mask:0xf bank_mask:0xf\n"
"s_nop 0\n"
"v_fmac_f32_dpp v120, v59, v53 row_newbcast:8 row_mask:0xf bank_mask:0xf\n"
"v_fmac_f32_dpp v121, v59, v53 row_newbcast:9 row_mask:0xf bank_mask:0xf\n"
"v_mfma_f32_32x32x16_bf16 v[96:111], v[80:83], v[144:147], v[96:111]\n"
"ds_read_b128 v[72:75], v50 offset:10240\n"
"buffer_load_dwordx4 a[148:151], v43, s[4:7], s76 offen offset:0 sc1\n"
"v_mul_f32 v22, v112, v54\n"
"v_mul_f32 v23, v113, v54\n"
"v_exp_f32_e64 v22, v22\n"
"v_mfma_f32_32x32x16_bf16 v[96:111], v[84:87], v[148:151], v[96:111]\n"
"ds_read_b128 v[76:79], v50 offset:11264\n"
"s_waitcnt lgkmcnt(4)\n"
"v_exp_f32 v23, v23\n"
"v_mul_f32 v112, v112, v120\n"
"v_mul_f32 v113, v113, v121\n"
"v_mfma_f32_32x32x16_bf16 v[96:111], v[88:91], v[152:155], v[96:111]\n"
"ds_read_b128 v[80:83], v50 offset:12288\n"
"buffer_load_dwordx4 a[152:155], v42, s[4:7], s78 offen offset:0 sc1\n"
"v_fma_f32 v22, v22, v55, v55\n"
"v_fma_f32 v23, v23, v55, v55\n"
"v_rcp_f32 v22, v22\n"
"v_mfma_f32_32x32x16_bf16 v[96:111], v[92:95], v[156:159], v[96:111]\n"
"ds_read_b128 v[84:87], v50 offset:13312\n"
"s_waitcnt lgkmcnt(4)\n"
"v_rcp_f32 v23, v23\n"
"v_mul_f32 v112, v112, v22\n"
"v_mul_f32_e64 v113, v113, v23\n"
"v_mfma_f32_32x32x16_bf16 v[96:111], v[64:67], v[160:163], v[96:111]\n"
"ds_read_b128 v[88:91], v50 offset:14336\n"
"buffer_load_dwordx4 a[156:159], v43, s[4:7], s78 offen offset:0 sc1\n"
"v_cvt_pk_bf16_f32 v28, v112, v113\n"
"v_fmac_f32_dpp v114, v59, v53 row_newbcast:2 row_mask:0xf bank_mask:0xf\n"
"v_fmac_f32_dpp v115, v59, v53 row_newbcast:3 row_mask:0xf bank_mask:0xf\n"
"v_fmac_f32_dpp v122, v59, v53 row_newbcast:10 row_mask:0xf bank_mask:0xf\n"
"v_mfma_f32_32x32x16_bf16 v[96:111], v[68:71], v[164:167], v[96:111]\n"
"ds_read_b128 v[92:95], v50 offset:15360\n"
"s_waitcnt lgkmcnt(4)\n"
"v_fmac_f32_dpp v123, v59, v53 row_newbcast:11 row_mask:0xf bank_mask:0xf\n"
"v_mul_f32 v22, v114, v54\n"
"v_mul_f32 v23, v115, v54\n"
"v_mfma_f32_32x32x16_bf16 v[96:111], v[72:75], v[168:171], v[96:111]\n"
"ds_read_b128 v[64:67], v50 offset:16384\n"
"v_exp_f32 v22, v22\n"
"v_exp_f32 v23, v23\n"
"v_mul_f32 v114, v114, v122\n"
"v_mfma_f32_32x32x16_bf16 v[96:111], v[76:79], v[172:175], v[96:111]\n"
"ds_read_b128 v[68:71], v50 offset:17408\n"
"s_waitcnt lgkmcnt(4)\n"
"buffer_load_dwordx4 a[160:163], v42, s[4:7], s72 offen offset:1024 sc1\n"
"v_mul_f32 v115, v115, v123\n"
"v_fma_f32 v22, v22, v55, v55\n"
"v_fma_f32 v23, v23, v55, v55\n"
"v_mfma_f32_32x32x16_bf16 v[96:111], v[80:83], v[176:179], v[96:111]\n"
"ds_read_b128 v[72:75], v50 offset:18432\n"
"v_rcp_f32 v22, v22\n"
"v_rcp_f32 v23, v23\n"
"v_mul_f32 v114, v114, v22\n"
"v_mfma_f32_32x32x16_bf16 v[96:111], v[84:87], v[180:183], v[96:111]\n"
"ds_read_b128 v[76:79], v50 offset:19456\n"
"s_waitcnt lgkmcnt(4)\n"
"buffer_load_dwordx4 a[164:167], v43, s[4:7], s72 offen offset:1024 sc1\n"
"v_mul_f32_e64 v115, v115, v23\n"
"v_cvt_pk_bf16_f32 v29, v114, v115\n"
"v_fmac_f32_dpp v116, v59, v53 row_newbcast:4 row_mask:0xf bank_mask:0xf\n"
"v_mfma_f32_32x32x16_bf16 v[96:111], v[88:91], v[184:187], v[96:111]\n"
"ds_read_b128 v[80:83], v50 offset:20480\n"
"v_fmac_f32_dpp v117, v59, v53 row_newbcast:5 row_mask:0xf bank_mask:0xf\n"
"v_fmac_f32_dpp v124, v59, v53 row_newbcast:12 row_mask:0xf bank_mask:0xf\n"
"v_fmac_f32_dpp v125, v59, v53 row_newbcast:13 row_mask:0xf bank_mask:0xf\n"
"v_mul_f32 v22, v116, v54\n"
"v_mul_f32 v23, v117, v54\n"
"v_mfma_f32_32x32x16_bf16 v[96:111], v[92:95], v[188:191], v[96:111]\n"
"ds_read_b128 v[84:87], v50 offset:21504\n"
"s_waitcnt lgkmcnt(4)\n"
"buffer_load_dwordx4 a[168:171], v42, s[4:7], s74 offen offset:1024 sc1\n"
"v_exp_f32 v22, v22\n"
"v_mfma_f32_32x32x16_bf16 v[96:111], v[64:67], v[192:195], v[96:111]\n"
"ds_read_b128 v[88:91], v50 offset:22528\n"
"v_exp_f32 v23, v23\n"
"v_mul_f32 v116, v116, v124\n"
"v_mul_f32_e64 v117, v117, v125\n"
"v_fma_f32 v22, v22, v55, v55\n"
"v_mfma_f32_32x32x16_bf16 v[96:111], v[68:71], v[196:199], v[96:111]\n"
"ds_read_b128 v[92:95], v50 offset:23552\n"
"s_waitcnt lgkmcnt(4)\n"
"v_fma_f32 v23, v23, v55, v55\n"
"v_rcp_f32 v22, v22\n"
"v_mfma_f32_32x32x16_bf16 v[96:111], v[72:75], v[200:203], v[96:111]\n"
"ds_read_b128 v[64:67], v50 offset:24576\n"
"buffer_load_dwordx4 a[172:175], v43, s[4:7], s74 offen offset:1024 sc1\n"
"v_rcp_f32 v23, v23\n"
"v_mul_f32 v116, v116, v22\n"
"v_mul_f32_e64 v117, v117, v23\n"
"v_mfma_f32_32x32x16_bf16 v[96:111], v[76:79], v[204:207], v[96:111]\n"
"ds_read_b128 v[68:71], v50 offset:25600\n"
"s_waitcnt lgkmcnt(4)\n"
"v_cvt_pk_bf16_f32 v30, v116, v117\n"
"s_nop 0\n"
"v_fmac_f32_dpp v118, v59, v53 row_newbcast:6 row_mask:0xf bank_mask:0xf\n"
"v_fmac_f32_dpp v119, v59, v53 row_newbcast:7 row_mask:0xf bank_mask:0xf\n"
"v_fmac_f32_dpp v126, v59, v53 row_newbcast:14 row_mask:0xf bank_mask:0xf\n"
"v_mfma_f32_32x32x16_bf16 v[96:111], v[80:83], v[208:211], v[96:111]\n"
"ds_read_b128 v[72:75], v50 offset:26624\n"
"buffer_load_dwordx4 a[176:179], v42, s[4:7], s76 offen offset:1024 sc1\n"
"v_fmac_f32_dpp v127, v59, v53 row_newbcast:15 row_mask:0xf bank_mask:0xf\n"
"v_mul_f32 v22, v118, v54\n"
"v_mul_f32 v23, v119, v54\n"
"v_mfma_f32_32x32x16_bf16 v[96:111], v[84:87], v[212:215], v[96:111]\n"
"ds_read_b128 v[76:79], v50 offset:27648\n"
"s_waitcnt lgkmcnt(4)\n"
"v_exp_f32 v22, v22\n"
"v_exp_f32_e64 v23, v23\n"
"v_mfma_f32_32x32x16_bf16 v[96:111], v[88:91], v[216:219], v[96:111]\n"
"ds_read_b128 v[80:83], v50 offset:28672\n"
"buffer_load_dwordx4 a[180:183], v43, s[4:7], s76 offen offset:1024 sc1\n"
"v_mul_f32 v118, v118, v126\n"
"v_mul_f32 v119, v119, v127\n"
"v_fma_f32 v22, v22, v55, v55\n"
"v_fma_f32 v23, v23, v55, v55\n"
"v_mfma_f32_32x32x16_bf16 v[96:111], v[92:95], v[220:223], v[96:111]\n"
"ds_read_b128 v[84:87], v50 offset:29696\n"
"s_waitcnt lgkmcnt(4)\n"
"v_rcp_f32 v22, v22\n"
"v_rcp_f32 v23, v23\n"
"v_mfma_f32_32x32x16_bf16 v[96:111], v[64:67], v[224:227], v[96:111]\n"
"ds_read_b128 v[88:91], v50 offset:30720\n"
"v_mul_f32 v118, v118, v22\n"
"v_mul_f32_e64 v119, v119, v23\n"
"v_cvt_pk_bf16_f32 v31, v118, v119\n"
"buffer_store_dwordx4 v[28:31], v2, s[4:7], s67 offen offset:3072 sc1\n"
"v_mfma_f32_32x32x16_bf16 v[96:111], v[68:71], v[228:231], v[96:111]\n"
"ds_read_b128 v[92:95], v50 offset:31744\n"
"s_waitcnt lgkmcnt(4)\n"
"buffer_load_dwordx4 a[184:187], v42, s[4:7], s78 offen offset:1024 sc1\n"
"v_mfma_f32_32x32x16_bf16 v[96:111], v[72:75], v[232:235], v[96:111]\n"
"s_waitcnt vmcnt(26)\n"
"s_barrier\n"
"ds_read_b128 v[64:67], v2 offset:0\n"
"v_mfma_f32_32x32x16_bf16 v[96:111], v[76:79], v[236:239], v[96:111]\n"
"ds_read_b128 v[68:71], v2 offset:1024\n"
"s_waitcnt lgkmcnt(4)\n"
"buffer_load_dwordx4 a[188:191], v43, s[4:7], s78 offen offset:1024 sc1\n"
"v_mfma_f32_32x32x16_bf16 v[96:111], v[80:83], v[240:243], v[96:111]\n"
"ds_read_b128 v[72:75], v2 offset:2048\n"
"v_mfma_f32_32x32x16_bf16 v[96:111], v[84:87], v[244:247], v[96:111]\n"
"ds_read_b128 v[76:79], v2 offset:3072\n"
"s_waitcnt lgkmcnt(4)\n"
"buffer_load_dwordx4 a[192:195], v42, s[4:7], s72 offen offset:2048 sc1\n"
"v_mfma_f32_32x32x16_bf16 v[96:111], v[88:91], v[248:251], v[96:111]\n"
"ds_read_b128 v[80:83], v2 offset:4096\n"
"v_mfma_f32_32x32x16_bf16 v[96:111], v[92:95], v[252:255], v[96:111]\n"
"ds_read_b128 v[84:87], v2 offset:5120\n"
"s_waitcnt lgkmcnt(4)\n"
"v_mfma_f32_32x32x16_bf16 v[112:127], v[64:67], v[128:131], 0\n"
"ds_read_b128 v[88:91], v2 offset:6144\n"
"buffer_load_dwordx4 a[196:199], v43, s[4:7], s72 offen offset:2048 sc1\n"
"v_mfma_f32_32x32x16_bf16 v[112:127], v[68:71], v[132:135], v[112:127]\n"
"ds_read_b128 v[92:95], v2 offset:7168\n"
"s_waitcnt lgkmcnt(4)\n"
"v_mfma_f32_32x32x16_bf16 v[112:127], v[72:75], v[136:139], v[112:127]\n"
"ds_read_b128 v[64:67], v2 offset:8192\n"
"buffer_load_dwordx4 a[200:203], v42, s[4:7], s74 offen offset:2048 sc1\n"
"v_mfma_f32_32x32x16_bf16 v[112:127], v[76:79], v[140:143], v[112:127]\n"
"ds_read_b128 v[68:71], v2 offset:9216\n"
"s_waitcnt lgkmcnt(4)\n"
"v_fmac_f32_dpp v96, v60, v53 row_newbcast:0 row_mask:0xf bank_mask:0xf\n"
"v_fmac_f32_dpp v97, v60, v53 row_newbcast:1 row_mask:0xf bank_mask:0xf\n"
"v_fmac_f32_dpp v104, v60, v53 row_newbcast:8 row_mask:0xf bank_mask:0xf\n"
"v_fmac_f32_dpp v105, v60, v53 row_newbcast:9 row_mask:0xf bank_mask:0xf\n"
"v_mfma_f32_32x32x16_bf16 v[112:127], v[80:83], v[144:147], v[112:127]\n"
"ds_read_b128 v[72:75], v2 offset:10240\n"
"buffer_load_dwordx4 a[204:207], v43, s[4:7], s74 offen offset:2048 sc1\n"
"v_mul_f32 v22, v96, v54\n"
"v_mul_f32 v23, v97, v54\n"
"v_exp_f32_e64 v22, v22\n"
"v_mfma_f32_32x32x16_bf16 v[112:127], v[84:87], v[148:151], v[112:127]\n"
"ds_read_b128 v[76:79], v2 offset:11264\n"
"s_waitcnt lgkmcnt(4)\n"
"buffer_load_dwordx2 v[6:7], v4, s[4:7], s37 offen offset:128 sc1\n"
"v_exp_f32 v23, v23\n"
"v_mul_f32_e64 v96, v96, v104\n"
"v_mfma_f32_32x32x16_bf16 v[112:127], v[88:91], v[152:155], v[112:127]\n"
"ds_read_b128 v[80:83], v2 offset:12288\n"
"v_mul_f32_e64 v97, v97, v105\n"
"v_fma_f32 v22, v22, v55, v55\n"
"v_fma_f32 v23, v23, v55, v55\n"
"v_rcp_f32 v22, v22\n"
"v_mfma_f32_32x32x16_bf16 v[112:127], v[92:95], v[156:159], v[112:127]\n"
"ds_read_b128 v[84:87], v2 offset:13312\n"
"s_waitcnt lgkmcnt(4)\n"
"buffer_load_dwordx4 a[208:211], v42, s[4:7], s76 offen offset:2048 sc1\n"
"v_rcp_f32 v23, v23\n"
"v_mul_f32 v96, v96, v22\n"
"v_mfma_f32_32x32x16_bf16 v[112:127], v[64:67], v[160:163], v[112:127]\n"
"ds_read_b128 v[88:91], v2 offset:14336\n"
"v_mul_f32_e64 v97, v97, v23\n"
"v_cvt_pk_bf16_f32 v24, v96, v97\n"
"v_fmac_f32_dpp v98, v60, v53 row_newbcast:2 row_mask:0xf bank_mask:0xf\n"
"v_fmac_f32_dpp v99, v60, v53 row_newbcast:3 row_mask:0xf bank_mask:0xf\n"
"v_fmac_f32_dpp v106, v60, v53 row_newbcast:10 row_mask:0xf bank_mask:0xf\n"
"v_mfma_f32_32x32x16_bf16 v[112:127], v[68:71], v[164:167], v[112:127]\n"
"ds_read_b128 v[92:95], v2 offset:15360\n"
"s_waitcnt lgkmcnt(4)\n"
"buffer_load_dwordx4 a[212:215], v43, s[4:7], s76 offen offset:2048 sc1\n"
"v_fmac_f32_dpp v107, v60, v53 row_newbcast:11 row_mask:0xf bank_mask:0xf\n"
"v_mul_f32 v22, v98, v54\n"
"v_mul_f32_e64 v23, v99, v54\n"
"v_mfma_f32_32x32x16_bf16 v[112:127], v[72:75], v[168:171], v[112:127]\n"
"ds_read_b128 v[64:67], v2 offset:16384\n"
"v_exp_f32 v22, v22\n"
"v_exp_f32 v23, v23\n"
"v_mul_f32_e64 v98, v98, v106\n"
"v_mfma_f32_32x32x16_bf16 v[112:127], v[76:79], v[172:175], v[112:127]\n"
"ds_read_b128 v[68:71], v2 offset:17408\n"
"s_waitcnt lgkmcnt(4)\n"
"buffer_load_dwordx4 a[216:219], v42, s[4:7], s78 offen offset:2048 sc1\n"
"v_mul_f32 v99, v99, v107\n"
"v_fma_f32 v22, v22, v55, v55\n"
"v_fma_f32 v23, v23, v55, v55\n"
"v_mfma_f32_32x32x16_bf16 v[112:127], v[80:83], v[176:179], v[112:127]\n"
"ds_read_b128 v[72:75], v2 offset:18432\n"
"v_rcp_f32 v22, v22\n"
"v_rcp_f32 v23, v23\n"
"v_mul_f32 v98, v98, v22\n"
"v_mfma_f32_32x32x16_bf16 v[112:127], v[84:87], v[180:183], v[112:127]\n"
"ds_read_b128 v[76:79], v2 offset:19456\n"
"s_waitcnt lgkmcnt(4)\n"
"v_mul_f32_e64 v99, v99, v23\n"
"v_cvt_pk_bf16_f32 v25, v98, v99\n"
"v_fmac_f32_dpp v100, v60, v53 row_newbcast:4 row_mask:0xf bank_mask:0xf\n"
"v_fmac_f32_dpp v101, v60, v53 row_newbcast:5 row_mask:0xf bank_mask:0xf\n"
"v_mfma_f32_32x32x16_bf16 v[112:127], v[88:91], v[184:187], v[112:127]\n"
"ds_read_b128 v[80:83], v2 offset:20480\n"
"buffer_load_dwordx4 a[220:223], v43, s[4:7], s78 offen offset:2048 sc1\n"
"v_fmac_f32_dpp v108, v60, v53 row_newbcast:12 row_mask:0xf bank_mask:0xf\n"
"v_fmac_f32_dpp v109, v60, v53 row_newbcast:13 row_mask:0xf bank_mask:0xf\n"
"v_mul_f32 v22, v100, v54\n"
"v_mul_f32 v23, v101, v54\n"
"v_mfma_f32_32x32x16_bf16 v[112:127], v[92:95], v[188:191], v[112:127]\n"
"ds_read_b128 v[84:87], v2 offset:21504\n"
"s_waitcnt lgkmcnt(4)\n"
"v_exp_f32 v22, v22\n"
"v_exp_f32 v23, v23\n"
"v_mfma_f32_32x32x16_bf16 v[112:127], v[64:67], v[192:195], v[112:127]\n"
"ds_read_b128 v[88:91], v2 offset:22528\n"
"v_mul_f32 v100, v100, v108\n"
"v_mul_f32_e64 v101, v101, v109\n"
"v_fma_f32 v22, v22, v55, v55\n"
"v_fma_f32 v23, v23, v55, v55\n"
"v_mfma_f32_32x32x16_bf16 v[112:127], v[68:71], v[196:199], v[112:127]\n"
"ds_read_b128 v[92:95], v2 offset:23552\n"
"s_waitcnt lgkmcnt(4)\n"
"v_rcp_f32 v22, v22\n"
"v_rcp_f32 v23, v23\n"
"v_mfma_f32_32x32x16_bf16 v[112:127], v[72:75], v[200:203], v[112:127]\n"
"ds_read_b128 v[64:67], v2 offset:24576\n"
"v_mul_f32 v100, v100, v22\n"
"v_mul_f32_e64 v101, v101, v23\n"
"v_cvt_pk_bf16_f32 v26, v100, v101\n"
"v_fmac_f32_dpp v102, v60, v53 row_newbcast:6 row_mask:0xf bank_mask:0xf\n"
"v_fmac_f32_dpp v103, v60, v53 row_newbcast:7 row_mask:0xf bank_mask:0xf\n"
"v_mfma_f32_32x32x16_bf16 v[112:127], v[76:79], v[204:207], v[112:127]\n"
"ds_read_b128 v[68:71], v2 offset:25600\n"
"s_waitcnt lgkmcnt(4)\n"
"v_fmac_f32_dpp v110, v60, v53 row_newbcast:14 row_mask:0xf bank_mask:0xf\n"
"v_fmac_f32_dpp v111, v60, v53 row_newbcast:15 row_mask:0xf bank_mask:0xf\n"
"v_mul_f32 v22, v102, v54\n"
"v_mul_f32 v23, v103, v54\n"
"v_mfma_f32_32x32x16_bf16 v[112:127], v[80:83], v[208:211], v[112:127]\n"
"ds_read_b128 v[72:75], v2 offset:26624\n"
"v_exp_f32 v22, v22\n"
"v_exp_f32 v23, v23\n"
"v_mul_f32 v102, v102, v110\n"
"v_mfma_f32_32x32x16_bf16 v[112:127], v[84:87], v[212:215], v[112:127]\n"
"ds_read_b128 v[76:79], v2 offset:27648\n"
"s_waitcnt lgkmcnt(4)\n"
"v_mul_f32 v103, v103, v111\n"
"v_fma_f32 v22, v22, v55, v55\n"
"v_fma_f32 v23, v23, v55, v55\n"
"v_mfma_f32_32x32x16_bf16 v[112:127], v[88:91], v[216:219], v[112:127]\n"
"ds_read_b128 v[80:83], v2 offset:28672\n"
"v_rcp_f32 v22, v22\n"
"v_rcp_f32 v23, v23\n"
"v_mul_f32 v102, v102, v22\n"
"v_mfma_f32_32x32x16_bf16 v[112:127], v[92:95], v[220:223], v[112:127]\n"
"ds_read_b128 v[84:87], v2 offset:29696\n"
"s_waitcnt lgkmcnt(4)\n"
"v_mul_f32_e64 v103, v103, v23\n"
"v_cvt_pk_bf16_f32 v27, v102, v103\n"
"buffer_store_dwordx4 v[24:27], v2, s[4:7], s68 offen offset:0 sc1\n"
"v_mfma_f32_32x32x16_bf16 v[112:127], v[64:67], v[224:227], v[112:127]\n"
"ds_read_b128 v[88:91], v2 offset:30720\n"
"v_mfma_f32_32x32x16_bf16 v[112:127], v[68:71], v[228:231], v[112:127]\n"
"ds_read_b128 v[92:95], v2 offset:31744\n"
"s_waitcnt lgkmcnt(4)\n"
"v_mfma_f32_32x32x16_bf16 v[112:127], v[72:75], v[232:235], v[112:127]\n"
"ds_read_b128 v[64:67], v2 offset:32768\n"
"v_mfma_f32_32x32x16_bf16 v[112:127], v[76:79], v[236:239], v[112:127]\n"
"ds_read_b128 v[68:71], v2 offset:33792\n"
"s_waitcnt lgkmcnt(4)\n"
"v_mfma_f32_32x32x16_bf16 v[112:127], v[80:83], v[240:243], v[112:127]\n"
"ds_read_b128 v[72:75], v2 offset:34816\n"
"v_mfma_f32_32x32x16_bf16 v[112:127], v[84:87], v[244:247], v[112:127]\n"
"ds_read_b128 v[76:79], v2 offset:35840\n"
"s_waitcnt lgkmcnt(4)\n"
"v_mfma_f32_32x32x16_bf16 v[112:127], v[88:91], v[248:251], v[112:127]\n"
"ds_read_b128 v[80:83], v2 offset:36864\n"
"s_waitcnt vmcnt(5)\n"
"v_cmp_ne_u64 vcc, s[62:63], v[6:7]\n"
"s_cbranch_vccnz L_p3\n"
"L_p3_back:\n"
"v_mfma_f32_32x32x16_bf16 v[112:127], v[92:95], v[252:255], v[112:127]\n"
"ds_read_b128 v[84:87], v2 offset:37888\n"
"s_waitcnt lgkmcnt(4)\n"
"v_mfma_f32_32x32x16_bf16 v[96:111], v[64:67], v[128:131], 0\n"
"ds_read_b128 v[88:91], v2 offset:38912\n"
"buffer_load_dwordx4 v[128:131], v44, s[4:7], s71 offen offset:0 sc1\n"
"v_mfma_f32_32x32x16_bf16 v[96:111], v[68:71], v[132:135], v[96:111]\n"
"ds_read_b128 v[92:95], v2 offset:39936\n"
"s_waitcnt lgkmcnt(4)\n"
"v_mfma_f32_32x32x16_bf16 v[96:111], v[72:75], v[136:139], v[96:111]\n"
"ds_read_b128 v[64:67], v2 offset:40960\n"
"buffer_load_dwordx4 v[132:135], v45, s[4:7], s71 offen offset:0 sc1\n"
"v_mfma_f32_32x32x16_bf16 v[96:111], v[76:79], v[140:143], v[96:111]\n"
"ds_read_b128 v[68:71], v2 offset:41984\n"
"s_waitcnt lgkmcnt(4)\n"
"v_fmac_f32_dpp v112, v61, v53 row_newbcast:0 row_mask:0xf bank_mask:0xf\n"
"v_fmac_f32_dpp v113, v61, v53 row_newbcast:1 row_mask:0xf bank_mask:0xf\n"
"s_nop 0\n"
"v_fmac_f32_dpp v120, v61, v53 row_newbcast:8 row_mask:0xf bank_mask:0xf\n"
"v_fmac_f32_dpp v121, v61, v53 row_newbcast:9 row_mask:0xf bank_mask:0xf\n"
"v_mfma_f32_32x32x16_bf16 v[96:111], v[80:83], v[144:147], v[96:111]\n"
"ds_read_b128 v[72:75], v2 offset:43008\n"
"buffer_load_dwordx4 v[136:139], v44, s[4:7], s73 offen offset:0 sc1\n"
"v_mul_f32 v22, v112, v54\n"
"v_mul_f32 v23, v113, v54\n"
"v_exp_f32_e64 v22, v22\n"
"v_mfma_f32_32x32x16_bf16 v[96:111], v[84:87], v[148:151], v[96:111]\n"
"ds_read_b128 v[76:79], v2 offset:44032\n"
"s_waitcnt lgkmcnt(4)\n"
"v_exp_f32 v23, v23\n"
"v_mul_f32 v112, v112, v120\n"
"v_mul_f32 v113, v113, v121\n"
"v_mfma_f32_32x32x16_bf16 v[96:111], v[88:91], v[152:155], v[96:111]\n"
"ds_read_b128 v[80:83], v2 offset:45056\n"
"buffer_load_dwordx4 v[140:143], v45, s[4:7], s73 offen offset:0 sc1\n"
"v_fma_f32 v22, v22, v55, v55\n"
"v_fma_f32 v23, v23, v55, v55\n"
"v_rcp_f32 v22, v22\n"
"v_mfma_f32_32x32x16_bf16 v[96:111], v[92:95], v[156:159], v[96:111]\n"
"ds_read_b128 v[84:87], v2 offset:46080\n"
"s_waitcnt lgkmcnt(4)\n"
"v_rcp_f32 v23, v23\n"
"v_mul_f32 v112, v112, v22\n"
"v_mul_f32_e64 v113, v113, v23\n"
"v_mfma_f32_32x32x16_bf16 v[96:111], v[64:67], v[160:163], v[96:111]\n"
"ds_read_b128 v[88:91], v2 offset:47104\n"
"buffer_load_dwordx4 v[144:147], v44, s[4:7], s75 offen offset:0 sc1\n"
"v_cvt_pk_bf16_f32 v28, v112, v113\n"
"v_fmac_f32_dpp v114, v61, v53 row_newbcast:2 row_mask:0xf bank_mask:0xf\n"
"v_fmac_f32_dpp v115, v61, v53 row_newbcast:3 row_mask:0xf bank_mask:0xf\n"
"v_fmac_f32_dpp v122, v61, v53 row_newbcast:10 row_mask:0xf bank_mask:0xf\n"
"v_mfma_f32_32x32x16_bf16 v[96:111], v[68:71], v[164:167], v[96:111]\n"
"ds_read_b128 v[92:95], v2 offset:48128\n"
"s_waitcnt lgkmcnt(4)\n"
"v_fmac_f32_dpp v123, v61, v53 row_newbcast:11 row_mask:0xf bank_mask:0xf\n"
"v_mul_f32 v22, v114, v54\n"
"v_mul_f32 v23, v115, v54\n"
"v_mfma_f32_32x32x16_bf16 v[96:111], v[72:75], v[168:171], v[96:111]\n"
"ds_read_b128 v[64:67], v2 offset:49152\n"
"buffer_load_dwordx4 v[148:151], v45, s[4:7], s75 offen offset:0 sc1\n"
"v_exp_f32 v22, v22\n"
"v_exp_f32 v23, v23\n"
"v_mfma_f32_32x32x16_bf16 v[96:111], v[76:79], v[172:175], v[96:111]\n"
"ds_read_b128 v[68:71], v2 offset:50176\n"
"s_waitcnt lgkmcnt(4)\n"
"v_mul_f32 v114, v114, v122\n"
"v_mul_f32 v115, v115, v123\n"
"v_fma_f32 v22, v22, v55, v55\n"
"v_fma_f32 v23, v23, v55, v55\n"
"v_mfma_f32_32x32x16_bf16 v[96:111], v[80:83], v[176:179], v[96:111]\n"
"ds_read_b128 v[72:75], v2 offset:51200\n"
"buffer_load_dwordx4 v[152:155], v44, s[4:7], s77 offen offset:0 sc1\n"
"v_rcp_f32 v22, v22\n"
"v_rcp_f32 v23, v23\n"
"v_mfma_f32_32x32x16_bf16 v[96:111], v[84:87], v[180:183], v[96:111]\n"
"ds_read_b128 v[76:79], v2 offset:52224\n"
"s_waitcnt lgkmcnt(4)\n"
"v_mul_f32 v114, v114, v22\n"
"v_mul_f32_e64 v115, v115, v23\n"
"v_cvt_pk_bf16_f32 v29, v114, v115\n"
"v_fmac_f32_dpp v116, v61, v53 row_newbcast:4 row_mask:0xf bank_mask:0xf\n"
"v_mfma_f32_32x32x16_bf16 v[96:111], v[88:91], v[184:187], v[96:111]\n"
"ds_read_b128 v[80:83], v2 offset:53248\n"
"buffer_load_dwordx4 v[156:159], v45, s[4:7], s77 offen offset:0 sc1\n"
"v_fmac_f32_dpp v117, v61, v53 row_newbcast:5 row_mask:0xf bank_mask:0xf\n"
"v_fmac_f32_dpp v124, v61, v53 row_newbcast:12 row_mask:0xf bank_mask:0xf\n"
"v_fmac_f32_dpp v125, v61, v53 row_newbcast:13 row_mask:0xf bank_mask:0xf\n"
"v_mul_f32_e64 v22, v116, v54\n"
"v_mfma_f32_32x32x16_bf16 v[96:111], v[92:95], v[188:191], v[96:111]\n"
"ds_read_b128 v[84:87], v2 offset:54272\n"
"s_waitcnt lgkmcnt(4)\n"
"v_mul_f32 v23, v117, v54\n"
"v_exp_f32_e64 v22, v22\n"
"v_mfma_f32_32x32x16_bf16 v[96:111], v[64:67], v[192:195], v[96:111]\n"
"ds_read_b128 v[88:91], v2 offset:55296\n"
"buffer_load_dwordx4 v[160:163], v44, s[4:7], s71 offen offset:1024 sc1\n"
"v_exp_f32 v23, v23\n"
"v_mul_f32 v116, v116, v124\n"
"v_mul_f32_e64 v117, v117, v125\n"
"v_mfma_f32_32x32x16_bf16 v[96:111], v[68:71], v[196:199], v[96:111]\n"
"ds_read_b128 v[92:95], v2 offset:56320\n"
"s_waitcnt lgkmcnt(4)\n"
"v_fma_f32 v22, v22, v55, v55\n"
"v_fma_f32 v23, v23, v55, v55\n"
"v_rcp_f32 v22, v22\n"
"v_mfma_f32_32x32x16_bf16 v[96:111], v[72:75], v[200:203], v[96:111]\n"
"ds_read_b128 v[64:67], v2 offset:57344\n"
"buffer_load_dwordx4 v[164:167], v45, s[4:7], s71 offen offset:1024 sc1\n"
"v_rcp_f32 v23, v23\n"
"v_mul_f32 v116, v116, v22\n"
"v_mul_f32 v117, v117, v23\n"
"v_mfma_f32_32x32x16_bf16 v[96:111], v[76:79], v[204:207], v[96:111]\n"
"ds_read_b128 v[68:71], v2 offset:58368\n"
"s_waitcnt lgkmcnt(4)\n"
"v_cvt_pk_bf16_f32 v30, v116, v117\n"
"v_fmac_f32_dpp v118, v61, v53 row_newbcast:6 row_mask:0xf bank_mask:0xf\n"
"v_fmac_f32_dpp v119, v61, v53 row_newbcast:7 row_mask:0xf bank_mask:0xf\n"
"v_fmac_f32_dpp v126, v61, v53 row_newbcast:14 row_mask:0xf bank_mask:0xf\n"
"v_mfma_f32_32x32x16_bf16 v[96:111], v[80:83], v[208:211], v[96:111]\n"
"ds_read_b128 v[72:75], v2 offset:59392\n"
"buffer_load_dwordx4 v[168:171], v44, s[4:7], s73 offen offset:1024 sc1\n"
"v_fmac_f32_dpp v127, v61, v53 row_newbcast:15 row_mask:0xf bank_mask:0xf\n"
"v_mul_f32 v22, v118, v54\n"
"v_mul_f32 v23, v119, v54\n"
"v_mfma_f32_32x32x16_bf16 v[96:111], v[84:87], v[212:215], v[96:111]\n"
"ds_read_b128 v[76:79], v2 offset:60416\n"
"s_waitcnt lgkmcnt(4)\n"
"v_exp_f32 v22, v22\n"
"v_exp_f32 v23, v23\n"
"v_mfma_f32_32x32x16_bf16 v[96:111], v[88:91], v[216:219], v[96:111]\n"
"ds_read_b128 v[80:83], v2 offset:61440\n"
"buffer_load_dwordx4 v[172:175], v45, s[4:7], s73 offen offset:1024 sc1\n"
"v_mul_f32 v118, v118, v126\n"
"v_mul_f32_e64 v119, v119, v127\n"
"v_fma_f32 v22, v22, v55, v55\n"
"v_fma_f32 v23, v23, v55, v55\n"
"v_mfma_f32_32x32x16_bf16 v[96:111], v[92:95], v[220:223], v[96:111]\n"
"ds_read_b128 v[84:87], v2 offset:62464\n"
"s_waitcnt lgkmcnt(4)\n"
"v_rcp_f32 v22, v22\n"
"v_rcp_f32_e64 v23, v23\n"
"v_mfma_f32_32x32x16_bf16 v[96:111], v[64:67], v[224:227], v[96:111]\n"
"ds_read_b128 v[88:91], v2 offset:63488\n"
"buffer_load_dwordx4 v[176:179], v44, s[4:7], s75 offen offset:1024 sc1\n"
"v_mul_f32 v118, v118, v22\n"
"v_mul_f32 v119, v119, v23\n"
"v_cvt_pk_bf16_f32 v31, v118, v119\n"
"buffer_store_dwordx4 v[28:31], v2, s[4:7], s68 offen offset:1024 sc1\n"
"v_mfma_f32_32x32x16_bf16 v[96:111], v[68:71], v[228:231], v[96:111]\n"
"ds_read_b128 v[92:95], v2 offset:64512\n"
"s_waitcnt lgkmcnt(4)\n"
"v_mfma_f32_32x32x16_bf16 v[96:111], v[72:75], v[232:235], v[96:111]\n"
"buffer_load_dwordx4 v[180:183], v45, s[4:7], s75 offen offset:1024 sc1\n"
"v_mfma_f32_32x32x16_bf16 v[96:111], v[76:79], v[236:239], v[96:111]\n"
"s_waitcnt lgkmcnt(2)\n"
"v_mfma_f32_32x32x16_bf16 v[96:111], v[80:83], v[240:243], v[96:111]\n"
"buffer_load_dwordx4 v[184:187], v44, s[4:7], s77 offen offset:1024 sc1\n"
"v_mfma_f32_32x32x16_bf16 v[96:111], v[84:87], v[244:247], v[96:111]\n"
"s_waitcnt lgkmcnt(0)\n"
"v_mfma_f32_32x32x16_bf16 v[96:111], v[88:91], v[248:251], v[96:111]\n"
"buffer_load_dwordx4 v[188:191], v45, s[4:7], s77 offen offset:1024 sc1\n"
"v_mfma_f32_32x32x16_bf16 v[96:111], v[92:95], v[252:255], v[96:111]\n"
"s_waitcnt vmcnt(15)\n"
"v_mfma_f32_32x32x16_bf16 v[112:127], a[0:3], v[128:131], 0\n"
"buffer_load_dwordx4 v[192:195], v44, s[4:7], s71 offen offset:2048 sc1\n"
"v_mfma_f32_32x32x16_bf16 a[224:239], a[0:3], v[132:135], 0\n"
"buffer_load_dwordx4 v[8:11], v51, s[4:7], s90 offen offset:0 sc1\n"
"s_waitcnt vmcnt(15)\n"
"v_mfma_f32_32x32x16_bf16 v[64:79], a[4:7], v[132:135], 0\n"
"buffer_load_dwordx4 v[196:199], v45, s[4:7], s71 offen offset:2048 sc1\n"
"s_nop 3\n"
"v_fmac_f32_dpp v96, v62, v53 row_newbcast:0 row_mask:0xf bank_mask:0xf\n"
"v_fmac_f32_dpp v97, v62, v53 row_newbcast:1 row_mask:0xf bank_mask:0xf\n"
"v_fmac_f32_dpp v104, v62, v53 row_newbcast:8 row_mask:0xf bank_mask:0xf\n"
"v_fmac_f32_dpp v105, v62, v53 row_newbcast:9 row_mask:0xf bank_mask:0xf\n"
"v_mul_f32 v22, v96, v54\n"
"v_mfma_f32_32x32x16_bf16 a[240:255], a[4:7], v[128:131], 0\n"
"buffer_load_dwordx4 v[12:15], v51, s[4:7], s90 offen offset:1024 sc1\n"
"v_mul_f32 v23, v97, v54\n"
"v_exp_f32 v22, v22\n"
"v_exp_f32 v23, v23\n"
"v_mfma_f32_32x32x16_bf16 v[112:127], a[8:11], v[136:139], v[112:127]\n"
"buffer_load_dwordx4 v[200:203], v44, s[4:7], s73 offen offset:2048 sc1\n"
"v_mul_f32 v96, v96, v104\n"
"v_mul_f32 v97, v97, v105\n"
"v_fma_f32 v22, v22, v55, v55\n"
"v_fma_f32 v23, v23, v55, v55\n"
"v_mfma_f32_32x32x16_bf16 a[224:239], a[8:11], v[140:143], a[224:239]\n"
"s_waitcnt vmcnt(16)\n"
"v_rcp_f32 v22, v22\n"
"v_rcp_f32 v23, v23\n"
"v_mul_f32 v96, v96, v22\n"
"v_mfma_f32_32x32x16_bf16 v[64:79], a[12:15], v[140:143], v[64:79]\n"
"buffer_load_dwordx4 v[204:207], v45, s[4:7], s73 offen offset:2048 sc1\n"
"v_mul_f32_e64 v97, v97, v23\n"
"v_cvt_pk_bf16_f32 v24, v96, v97\n"
"v_fmac_f32_dpp v98, v62, v53 row_newbcast:2 row_mask:0xf bank_mask:0xf\n"
"v_fmac_f32_dpp v99, v62, v53 row_newbcast:3 row_mask:0xf bank_mask:0xf\n"
"v_fmac_f32_dpp v106, v62, v53 row_newbcast:10 row_mask:0xf bank_mask:0xf\n"
"v_mfma_f32_32x32x16_bf16 a[240:255], a[12:15], v[136:139], a[240:255]\n"
"v_fmac_f32_dpp v107, v62, v53 row_newbcast:11 row_mask:0xf bank_mask:0xf\n"
"v_mul_f32 v22, v98, v54\n"
"v_mul_f32 v23, v99, v54\n"
"v_exp_f32_e64 v22, v22\n"
"v_mfma_f32_32x32x16_bf16 v[112:127], a[16:19], v[144:147], v[112:127]\n"
"buffer_load_dwordx4 v[208:211], v44, s[4:7], s75 offen offset:2048 sc1\n"
"v_exp_f32 v23, v23\n"
"v_mul_f32 v98, v98, v106\n"
"v_mul_f32_e64 v99, v99, v107\n"
"v_fma_f32 v22, v22, v55, v55\n"
"v_mfma_f32_32x32x16_bf16 a[224:239], a[16:19], v[148:151], a[224:239]\n"
"s_waitcnt vmcnt(16)\n"
"v_fma_f32 v23, v23, v55, v55\n"
"v_rcp_f32 v22, v22\n"
"v_rcp_f32 v23, v23\n"
"v_mfma_f32_32x32x16_bf16 v[64:79], a[20:23], v[148:151], v[64:79]\n"
"buffer_load_dwordx4 v[212:215], v45, s[4:7], s75 offen offset:2048 sc1\n"
"v_mul_f32 v98, v98, v22\n"
"v_mul_f32_e64 v99, v99, v23\n"
"v_cvt_pk_bf16_f32 v25, v98, v99\n"
"v_fmac_f32_dpp v100, v62, v53 row_newbcast:4 row_mask:0xf bank_mask:0xf\n"
"v_fmac_f32_dpp v101, v62, v53 row_newbcast:5 row_mask:0xf bank_mask:0xf\n"
"v_mfma_f32_32x32x16_bf16 a[240:255], a[20:23], v[144:147], a[240:255]\n"
"v_fmac_f32_dpp v108, v62, v53 row_newbcast:12 row_mask:0xf bank_mask:0xf\n"
"v_fmac_f32_dpp v109, v62, v53 row_newbcast:13 row_mask:0xf bank_mask:0xf\n"
"v_mul_f32 v22, v100, v54\n"
"v_mul_f32 v23, v101, v54\n"
"v_exp_f32_e64 v22, v22\n"
"v_mfma_f32_32x32x16_bf16 v[112:127], a[24:27], v[152:155], v[112:127]\n"
"buffer_load_dwordx4 v[216:219], v44, s[4:7], s77 offen offset:2048 sc1\n"
"v_exp_f32 v23, v23\n"
"v_mul_f32 v100, v100, v108\n"
"v_mul_f32_e64 v101, v101, v109\n"
"v_fma_f32 v22, v22, v55, v55\n"
"v_mfma_f32_32x32x16_bf16 a[224:239], a[24:27], v[156:159], a[224:239]\n"
"s_waitcnt vmcnt(16)\n"
"v_fma_f32 v23, v23, v55, v55\n"
"v_rcp_f32 v22, v22\n"
"v_rcp_f32 v23, v23\n"
"v_mfma_f32_32x32x16_bf16 v[64:79], a[28:31], v[156:159], v[64:79]\n"
"buffer_load_dwordx4 v[220:223], v45, s[4:7], s77 offen offset:2048 sc1\n"
"v_mul_f32 v100, v100, v22\n"
"v_mul_f32_e64 v101, v101, v23\n"
"v_cvt_pk_bf16_f32 v26, v100, v101\n"
"v_fmac_f32_dpp v102, v62, v53 row_newbcast:6 row_mask:0xf bank_mask:0xf\n"
"v_fmac_f32_dpp v103, v62, v53 row_newbcast:7 row_mask:0xf bank_mask:0xf\n"
"v_mfma_f32_32x32x16_bf16 a[240:255], a[28:31], v[152:155], a[240:255]\n"
"v_fmac_f32_dpp v110, v62, v53 row_newbcast:14 row_mask:0xf bank_mask:0xf\n"
"v_fmac_f32_dpp v111, v62, v53 row_newbcast:15 row_mask:0xf bank_mask:0xf\n"
"v_mul_f32 v22, v102, v54\n"
"v_mul_f32 v23, v103, v54\n"
"v_exp_f32_e64 v22, v22\n"
"v_mfma_f32_32x32x16_bf16 v[112:127], a[32:35], v[160:163], v[112:127]\n"
"buffer_load_dwordx4 v[224:227], v44, s[4:7], s71 offen offset:3072 sc1\n"
"v_exp_f32 v23, v23\n"
"v_mul_f32 v102, v102, v110\n"
"v_mul_f32_e64 v103, v103, v111\n"
"v_fma_f32 v22, v22, v55, v55\n"
"v_mfma_f32_32x32x16_bf16 a[224:239], a[32:35], v[164:167], a[224:239]\n"
"s_waitcnt vmcnt(16)\n"
"v_fma_f32 v23, v23, v55, v55\n"
"v_rcp_f32 v22, v22\n"
"v_rcp_f32 v23, v23\n"
"v_mfma_f32_32x32x16_bf16 v[64:79], a[36:39], v[164:167], v[64:79]\n"
"buffer_load_dwordx4 v[228:231], v45, s[4:7], s71 offen offset:3072 sc1\n"
"v_mul_f32 v102, v102, v22\n"
"v_mul_f32_e64 v103, v103, v23\n"
"v_cvt_pk_bf16_f32 v27, v102, v103\n"
"buffer_store_dwordx4 v[24:27], v2, s[4:7], s68 offen offset:2048 sc1\n"
"v_mfma_f32_32x32x16_bf16 a[240:255], a[36:39], v[160:163], a[240:255]\n"
"v_mfma_f32_32x32x16_bf16 v[112:127], a[40:43], v[168:171], v[112:127]\n"
"buffer_load_dwordx4 v[232:235], v44, s[4:7], s73 offen offset:3072 sc1\n"
"v_mfma_f32_32x32x16_bf16 a[224:239], a[40:43], v[172:175], a[224:239]\n"
"s_waitcnt vmcnt(16)\n"
"v_mfma_f32_32x32x16_bf16 v[64:79], a[44:47], v[172:175], v[64:79]\n"
"s_nop 0\n"
"buffer_load_dwordx4 v[236:239], v45, s[4:7], s73 offen offset:3072 sc1\n"
"v_mfma_f32_32x32x16_bf16 a[240:255], a[44:47], v[168:171], a[240:255]\n"
"v_mfma_f32_32x32x16_bf16 v[112:127], a[48:51], v[176:179], v[112:127]\n"
"buffer_load_dwordx4 v[240:243], v44, s[4:7], s75 offen offset:3072 sc1\n"
"v_mfma_f32_32x32x16_bf16 a[224:239], a[48:51], v[180:183], a[224:239]\n"
"s_waitcnt vmcnt(16)\n"
"v_mfma_f32_32x32x16_bf16 v[64:79], a[52:55], v[180:183], v[64:79]\n"
"buffer_load_dwordx4 v[244:247], v45, s[4:7], s75 offen offset:3072 sc1\n"
"v_mfma_f32_32x32x16_bf16 a[240:255], a[52:55], v[176:179], a[240:255]\n"
"v_mfma_f32_32x32x16_bf16 v[112:127], a[56:59], v[184:187], v[112:127]\n"
"buffer_load_dwordx4 v[248:251], v44, s[4:7], s77 offen offset:3072 sc1\n"
"v_mfma_f32_32x32x16_bf16 a[224:239], a[56:59], v[188:191], a[224:239]\n"
"s_waitcnt vmcnt(15)\n"
"v_mfma_f32_32x32x16_bf16 v[64:79], a[60:63], v[188:191], v[64:79]\n"
"buffer_load_dwordx4 v[252:255], v45, s[4:7], s77 offen offset:3072 sc1\n"
"v_mfma_f32_32x32x16_bf16 a[240:255], a[60:63], v[184:187], a[240:255]\n"
"v_mfma_f32_32x32x16_bf16 v[112:127], a[64:67], v[192:195], v[112:127]\n"
"buffer_load_dwordx4 v[128:131], v44, s[4:7], s72 offen offset:0 sc1\n"
"v_mfma_f32_32x32x16_bf16 a[224:239], a[64:67], v[196:199], a[224:239]\n"
"s_waitcnt vmcnt(14)\n"
"v_mfma_f32_32x32x16_bf16 v[64:79], a[68:71], v[196:199], v[64:79]\n"
"s_nop 0\n"
"buffer_load_dwordx4 v[132:135], v45, s[4:7], s72 offen offset:0 sc1\n"
"v_mfma_f32_32x32x16_bf16 a[240:255], a[68:71], v[192:195], a[240:255]\n"
"v_mfma_f32_32x32x16_bf16 v[112:127], a[72:75], v[200:203], v[112:127]\n"
"buffer_load_dwordx4 v[136:139], v44, s[4:7], s74 offen offset:0 sc1\n"
"v_mfma_f32_32x32x16_bf16 a[224:239], a[72:75], v[204:207], a[224:239]\n"
"s_waitcnt vmcnt(14)\n"
"v_mfma_f32_32x32x16_bf16 v[64:79], a[76:79], v[204:207], v[64:79]\n"
"buffer_load_dwordx4 v[140:143], v45, s[4:7], s74 offen offset:0 sc1\n"
"v_mfma_f32_32x32x16_bf16 a[240:255], a[76:79], v[200:203], a[240:255]\n"
"v_mfma_f32_32x32x16_bf16 v[112:127], a[80:83], v[208:211], v[112:127]\n"
"buffer_load_dwordx4 v[144:147], v44, s[4:7], s76 offen offset:0 sc1\n"
"s_xor_b32 s34, s3, 1\n"
"s_mul_i32 s34, s34, 12288\n"
"s_add_u32 s34, s34, 65536\n"
"v_add_u32_e64 v20, s34, v2\n"
"s_xor_b32 s34, s3, 2\n"
"v_mfma_f32_32x32x16_bf16 a[224:239], a[80:83], v[212:215], a[224:239]\n"
"s_waitcnt vmcnt(14)\n"
"s_mul_i32 s34, s34, 12288\n"
"s_add_u32 s34, s34, 69632\n"
"v_add_u32 v21, s34, v2\n"
"s_xor_b32 s34, s3, 3\n"
"s_mul_i32 s34, s34, 12288\n"
"v_mfma_f32_32x32x16_bf16 v[64:79], a[84:87], v[212:215], v[64:79]\n"
"buffer_load_dwordx4 v[148:151], v45, s[4:7], s76 offen offset:0 sc1\n"
"s_add_u32 s34, s34, 73728\n"
"v_add_u32_e64 v22, s34, v2\n"
"s_mul_i32 s34, s3, 12288\n"
"s_add_u32 s34, s34, 65536\n"
"v_add_u32_e64 v19, s34, v2\n"
"v_mfma_f32_32x32x16_bf16 a[240:255], a[84:87], v[208:211], a[240:255]\n"
"v_mfma_f32_32x32x16_bf16 v[112:127], a[88:91], v[216:219], v[112:127]\n"
"buffer_load_dwordx4 v[152:155], v44, s[4:7], s78 offen offset:0 sc1\n"
"v_mfma_f32_32x32x16_bf16 a[224:239], a[88:91], v[220:223], a[224:239]\n"
"s_waitcnt vmcnt(14)\n"
"s_nop 0\n"
"v_mfma_f32_32x32x16_bf16 v[64:79], a[92:95], v[220:223], v[64:79]\n"
"buffer_load_dwordx4 v[156:159], v45, s[4:7], s78 offen offset:0 sc1\n"
"v_mfma_f32_32x32x16_bf16 a[240:255], a[92:95], v[216:219], a[240:255]\n"
"v_mfma_f32_32x32x16_bf16 v[112:127], a[96:99], v[224:227], v[112:127]\n"
"buffer_load_dwordx4 v[160:163], v44, s[4:7], s72 offen offset:1024 sc1\n"
"v_mfma_f32_32x32x16_bf16 a[224:239], a[96:99], v[228:231], a[224:239]\n"
"s_waitcnt vmcnt(13)\n"
"v_mfma_f32_32x32x16_bf16 v[64:79], a[100:103], v[228:231], v[64:79]\n"
"buffer_load_dwordx4 v[164:167], v45, s[4:7], s72 offen offset:1024 sc1\n"
"v_mfma_f32_32x32x16_bf16 a[240:255], a[100:103], v[224:227], a[240:255]\n"
"v_mfma_f32_32x32x16_bf16 v[112:127], a[104:107], v[232:235], v[112:127]\n"
"buffer_load_dwordx4 v[168:171], v44, s[4:7], s74 offen offset:1024 sc1\n"
"v_permlane16_swap_b32 v8, v9\n"
"v_permlane16_swap_b32 v10, v11\n"
"v_mov_b32 v32, v8\n"
"v_mov_b32 v33, v9\n"
"v_permlane16_swap_b32 v12, v13\n"
"v_mfma_f32_32x32x16_bf16 a[224:239], a[104:107], v[236:239], a[224:239]\n"
"s_waitcnt vmcnt(13)\n"
"v_permlane16_swap_b32 v14, v15\n"
"v_mov_b32 v34, v12\n"
"v_mov_b32 v35, v13\n"
"v_mov_b32_dpp v8, v10 row_ror:8 row_mask:0xf bank_mask:0xc\n"
"v_mov_b32_dpp v9, v11 row_ror:8 row_mask:0xf bank_mask:0xc\n"
"v_mfma_f32_32x32x16_bf16 v[64:79], a[108:111], v[236:239], v[64:79]\n"
"buffer_load_dwordx4 v[172:175], v45, s[4:7], s74 offen offset:1024 sc1\n"
"v_mov_b32_dpp v10, v32 row_ror:8 row_mask:0xf bank_mask:0x3\n"
"v_mov_b32_dpp v11, v33 row_ror:8 row_mask:0xf bank_mask:0x3\n"
"v_mov_b32_dpp v12, v14 row_ror:8 row_mask:0xf bank_mask:0xc\n"
"v_mov_b32_dpp v13, v15 row_ror:8 row_mask:0xf bank_mask:0xc\n"
"v_mov_b32_dpp v14, v34 row_ror:8 row_mask:0xf bank_mask:0x3\n"
"v_mfma_f32_32x32x16_bf16 a[240:255], a[108:111], v[232:235], a[240:255]\n"
"v_mov_b32_dpp v15, v35 row_ror:8 row_mask:0xf bank_mask:0x3\n"
"v_lshlrev_b32 v80, 16, v8\n"
"v_lshlrev_b32 v82, 16, v9\n"
"v_lshlrev_b32 v84, 16, v10\n"
"v_lshlrev_b32 v86, 16, v11\n"
"v_lshlrev_b32_e64 v88, 16, v12\n"
"v_mfma_f32_32x32x16_bf16 v[112:127], a[112:115], v[240:243], v[112:127]\n"
"buffer_load_dwordx4 v[176:179], v44, s[4:7], s76 offen offset:1024 sc1\n"
"v_lshlrev_b32 v90, 16, v13\n"
"v_lshlrev_b32 v92, 16, v14\n"
"v_lshlrev_b32_e64 v94, 16, v15\n"
"v_and_b32 v81, 0xffff0000, v8\n"
"v_and_b32 v83, 0xffff0000, v9\n"
"v_mfma_f32_32x32x16_bf16 a[224:239], a[112:115], v[244:247], a[224:239]\n"
"s_waitcnt vmcnt(13)\n"
"v_and_b32 v85, 0xffff0000, v10\n"
"s_nop 0\n"
"v_and_b32 v87, 0xffff0000, v11\n"
"v_and_b32 v89, 0xffff0000, v12\n"
"v_and_b32 v91, 0xffff0000, v13\n"
"v_and_b32 v93, 0xffff0000, v14\n"
"v_mfma_f32_32x32x16_bf16 v[64:79], a[116:119], v[244:247], v[64:79]\n"
"buffer_load_dwordx4 v[180:183], v45, s[4:7], s76 offen offset:1024 sc1\n"
"v_and_b32 v95, 0xffff0000, v15\n"
"v_mfma_f32_32x32x16_bf16 a[240:255], a[116:119], v[240:243], a[240:255]\n"
"v_mfma_f32_32x32x16_bf16 v[112:127], a[120:123], v[248:251], v[112:127]\n"
"buffer_load_dwordx4 v[184:187], v44, s[4:7], s78 offen offset:1024 sc1\n"
"v_mfma_f32_32x32x16_bf16 a[224:239], a[120:123], v[252:255], a[224:239]\n"
"s_waitcnt vmcnt(13)\n"
"s_nop 0\n"
"v_mfma_f32_32x32x16_bf16 v[64:79], a[124:127], v[252:255], v[64:79]\n"
"buffer_load_dwordx4 v[188:191], v45, s[4:7], s78 offen offset:1024 sc1\n"
"v_mfma_f32_32x32x16_bf16 a[240:255], a[124:127], v[248:251], a[240:255]\n"
"v_mfma_f32_32x32x16_bf16 v[112:127], a[128:131], v[128:131], v[112:127]\n"
"buffer_load_dwordx4 v[192:195], v44, s[4:7], s72 offen offset:2048 sc1\n"
"v_mfma_f32_32x32x16_bf16 a[224:239], a[128:131], v[132:135], a[224:239]\n"
"s_waitcnt vmcnt(13)\n"
"v_mfma_f32_32x32x16_bf16 v[64:79], a[132:135], v[132:135], v[64:79]\n"
"s_nop 0\n"
"buffer_load_dwordx4 v[196:199], v45, s[4:7], s72 offen offset:2048 sc1\n"
"v_mfma_f32_32x32x16_bf16 a[240:255], a[132:135], v[128:131], a[240:255]\n"
"v_mfma_f32_32x32x16_bf16 v[112:127], a[136:139], v[136:139], v[112:127]\n"
"buffer_load_dwordx4 v[200:203], v44, s[4:7], s74 offen offset:2048 sc1\n"
"v_mfma_f32_32x32x16_bf16 a[224:239], a[136:139], v[140:143], a[224:239]\n"
"s_waitcnt vmcnt(13)\n"
"v_mfma_f32_32x32x16_bf16 v[64:79], a[140:143], v[140:143], v[64:79]\n"
"buffer_load_dwordx4 v[204:207], v45, s[4:7], s74 offen offset:2048 sc1\n"
"v_mfma_f32_32x32x16_bf16 a[240:255], a[140:143], v[136:139], a[240:255]\n"
"v_mfma_f32_32x32x16_bf16 v[112:127], a[144:147], v[144:147], v[112:127]\n"
"buffer_load_dwordx4 v[208:211], v44, s[4:7], s76 offen offset:2048 sc1\n"
"v_mfma_f32_32x32x16_bf16 a[224:239], a[144:147], v[148:151], a[224:239]\n"
"s_waitcnt vmcnt(13)\n"
"v_mfma_f32_32x32x16_bf16 v[64:79], a[148:151], v[148:151], v[64:79]\n"
"buffer_load_dwordx4 v[212:215], v45, s[4:7], s76 offen offset:2048 sc1\n"
"v_mfma_f32_32x32x16_bf16 a[240:255], a[148:151], v[144:147], a[240:255]\n"
"v_mfma_f32_32x32x16_bf16 v[112:127], a[152:155], v[152:155], v[112:127]\n"
"buffer_load_dwordx4 v[216:219], v44, s[4:7], s78 offen offset:2048 sc1\n"
"v_mfma_f32_32x32x16_bf16 a[224:239], a[152:155], v[156:159], a[224:239]\n"
"s_waitcnt vmcnt(13)\n"
"v_max3_u32 v18, v160, v161, v162\n"
"v_max3_u32 v18, v18, v163, v164\n"
"v_max3_u32 v18, v18, v165, v166\n"
"v_max_u32 v18, v18, v167\n"
"v_cmp_eq_u32 vcc, -1, v18\n"
"s_cbranch_vccnz L_hm20\n"
"L_hm20_back:\n"
"v_mfma_f32_32x32x16_bf16 v[64:79], a[156:159], v[156:159], v[64:79]\n"
"buffer_load_dwordx4 v[220:223], v45, s[4:7], s78 offen offset:2048 sc1\n"
"v_mfma_f32_32x32x16_bf16 a[240:255], a[156:159], v[152:155], a[240:255]\n"
"v_mfma_f32_32x32x16_bf16 v[112:127], a[160:163], v[160:163], v[112:127]\n"
"v_mfma_f32_32x32x16_bf16 a[224:239], a[160:163], v[164:167], a[224:239]\n"
"s_waitcnt vmcnt(12)\n"
"v_max3_u32 v18, v168, v169, v170\n"
"v_max3_u32 v18, v18, v171, v172\n"
"v_max3_u32 v18, v18, v173, v174\n"
"v_max_u32 v18, v18, v175\n"
"v_cmp_eq_u32 vcc, -1, v18\n"
"s_cbranch_vccnz L_hm21\n"
"L_hm21_back:\n"
"v_mfma_f32_32x32x16_bf16 v[64:79], a[164:167], v[164:167], v[64:79]\n"
"v_mfma_f32_32x32x16_bf16 a[240:255], a[164:167], v[160:163], a[240:255]\n"
"v_mfma_f32_32x32x16_bf16 v[112:127], a[168:171], v[168:171], v[112:127]\n"
"v_mfma_f32_32x32x16_bf16 a[224:239], a[168:171], v[172:175], a[224:239]\n"
"s_waitcnt vmcnt(10)\n"
"v_max3_u32 v18, v176, v177, v178\n"
"v_max3_u32 v18, v18, v179, v180\n"
"v_max3_u32 v18, v18, v181, v182\n"
"v_max_u32 v18, v18, v183\n"
"v_cmp_eq_u32 vcc, -1, v18\n"
"s_cbranch_vccnz L_hm22\n"
"L_hm22_back:\n"
"v_mfma_f32_32x32x16_bf16 v[64:79], a[172:175], v[172:175], v[64:79]\n"
"v_mfma_f32_32x32x16_bf16 a[240:255], a[172:175], v[168:171], a[240:255]\n"
"v_mfma_f32_32x32x16_bf16 v[112:127], a[176:179], v[176:179], v[112:127]\n"
"v_mfma_f32_32x32x16_bf16 a[224:239], a[176:179], v[180:183], a[224:239]\n"
"s_waitcnt vmcnt(8)\n"
"v_max3_u32 v18, v184, v185, v186\n"
"v_max3_u32 v18, v18, v187, v188\n"
"v_max3_u32 v18, v18, v189, v190\n"
"v_max_u32 v18, v18, v191\n"
"v_cmp_eq_u32 vcc, -1, v18\n"
"s_cbranch_vccnz L_hm23\n"
"L_hm23_back:\n"
"v_mfma_f32_32x32x16_bf16 v[64:79], a[180:183], v[180:183], v[64:79]\n"
"v_mfma_f32_32x32x16_bf16 a[240:255], a[180:183], v[176:179], a[240:255]\n"
"v_mfma_f32_32x32x16_bf16 v[112:127], a[184:187], v[184:187], v[112:127]\n"
"s_waitcnt vmcnt(6)\n"
"v_max3_u32 v18, v192, v193, v194\n"
"v_max3_u32 v18, v18, v195, v196\n"
"v_max3_u32 v18, v18, v197, v198\n"
"v_max_u32 v18, v18, v199\n"
"v_cmp_eq_u32 vcc, -1, v18\n"
"s_cbranch_vccnz L_hm24\n"
"L_hm24_back:\n"
"v_mfma_f32_32x32x16_bf16 a[224:239], a[184:187], v[188:191], a[224:239]\n"
"s_waitcnt vmcnt(4)\n"
"v_max3_u32 v18, v200, v201, v202\n"
"v_max3_u32 v18, v18, v203, v204\n"
"v_max3_u32 v18, v18, v205, v206\n"
"v_max_u32 v18, v18, v207\n"
"v_cmp_eq_u32_e64 vcc, -1, v18\n"
"s_cbranch_vccnz L_hm25\n"
"L_hm25_back:\n"
"v_mfma_f32_32x32x16_bf16 v[64:79], a[188:191], v[188:191], v[64:79]\n"
"s_waitcnt vmcnt(2)\n"
"v_max3_u32 v18, v208, v209, v210\n"
"v_max3_u32 v18, v18, v211, v212\n"
"v_max3_u32 v18, v18, v213, v214\n"
"v_max_u32 v18, v18, v215\n"
"v_cmp_eq_u32_e64 vcc, -1, v18\n"
"s_cbranch_vccnz L_hm26\n"
"L_hm26_back:\n"
"v_mfma_f32_32x32x16_bf16 a[240:255], a[188:191], v[184:187], a[240:255]\n"
"s_waitcnt vmcnt(0)\n"
"v_max3_u32 v18, v216, v217, v218\n"
"v_max3_u32 v18, v18, v219, v220\n"
"v_max3_u32 v18, v18, v221, v222\n"
"v_max_u32 v18, v18, v223\n"
"v_cmp_eq_u32 vcc, -1, v18\n"
"s_cbranch_vccnz L_hm27\n"
"L_hm27_back:\n"
"v_mfma_f32_32x32x16_bf16 v[64:79], a[196:199], v[196:199], v[64:79]\n"
"v_mfma_f32_32x32x16_bf16 v[64:79], a[204:207], v[204:207], v[64:79]\n"
"v_mfma_f32_32x32x16_bf16 v[64:79], a[212:215], v[212:215], v[64:79]\n"
"v_mfma_f32_32x32x16_bf16 v[64:79], a[220:223], v[220:223], v[64:79]\n"
"v_mfma_f32_32x32x16_bf16 a[240:255], a[196:199], v[192:195], a[240:255]\n"
"s_nop 4\n"
"v_mfma_f32_32x32x16_bf16 a[240:255], a[204:207], v[200:203], a[240:255]\n"
"s_nop 4\n"
"v_mfma_f32_32x32x16_bf16 a[240:255], a[212:215], v[208:211], a[240:255]\n"
"ds_write_b128 v22, v[64:67] offset:0\n"
"ds_write_b128 v22, v[68:71] offset:1024\n"
"v_mfma_f32_32x32x16_bf16 a[240:255], a[220:223], v[216:219], a[240:255]\n"
"ds_write_b128 v22, v[72:75] offset:2048\n"
"ds_write_b128 v22, v[76:79] offset:3072\n"
"v_mfma_f32_32x32x16_bf16 a[224:239], a[192:195], v[196:199], a[224:239]\n"
"s_nop 4\n"
"v_mfma_f32_32x32x16_bf16 a[224:239], a[200:203], v[204:207], a[224:239]\n"
"s_nop 4\n"
"v_mfma_f32_32x32x16_bf16 a[224:239], a[208:211], v[212:215], a[224:239]\n"
"ds_write_b128 v21, a[240:243] offset:0\n"
"ds_write_b128 v21, a[244:247] offset:1024\n"
"v_mfma_f32_32x32x16_bf16 a[224:239], a[216:219], v[220:223], a[224:239]\n"
"ds_write_b128 v21, a[248:251] offset:2048\n"
"ds_write_b128 v21, a[252:255] offset:3072\n"
"v_mfma_f32_32x32x16_bf16 v[112:127], a[192:195], v[192:195], v[112:127]\n"
"s_nop 4\n"
"v_mfma_f32_32x32x16_bf16 v[112:127], a[200:203], v[200:203], v[112:127]\n"
"s_nop 4\n"
"v_mfma_f32_32x32x16_bf16 v[112:127], a[208:211], v[208:211], v[112:127]\n"
"ds_write_b128 v20, a[224:227] offset:0\n"
"ds_write_b128 v20, a[228:231] offset:1024\n"
"v_mfma_f32_32x32x16_bf16 v[112:127], a[216:219], v[216:219], v[112:127]\n"
"ds_write_b128 v20, a[232:235] offset:2048\n"
"ds_write_b128 v20, a[236:239] offset:3072\n"
"v_accvgpr_read_b32 v18, a240\n"
"s_nop 0\n"
"v_cmp_u_f32 vcc, v18, v64\n"
"s_cbranch_vccnz L_redo\n"
"L_redo_back:\n"
"s_waitcnt lgkmcnt(0)\n"
"s_barrier\n"
"ds_read_b128 v[128:131], v19 offset:0\n"
"ds_read_b128 v[132:135], v19 offset:4096\n"
"ds_read_b128 v[136:139], v19 offset:8192\n"
"s_nop 0\n"
"ds_read_b128 v[140:143], v19 offset:1024\n"
"ds_read_b128 v[144:147], v19 offset:5120\n"
"ds_read_b128 v[148:151], v19 offset:9216\n"
"ds_read_b128 v[152:155], v19 offset:2048\n"
"ds_read_b128 v[156:159], v19 offset:6144\n"
"ds_read_b128 v[160:163], v19 offset:10240\n"
"ds_read_b128 v[164:167], v19 offset:3072\n"
"ds_read_b128 v[168:171], v19 offset:7168\n"
"ds_read_b128 v[172:175], v19 offset:11264\n"
"v_pk_add_f32 v[112:113], v[112:113], v[80:81]\n"
"v_pk_add_f32 v[114:115], v[114:115], v[82:83]\n"
"v_pk_add_f32 v[116:117], v[116:117], v[84:85]\n"
"v_pk_add_f32 v[118:119], v[118:119], v[86:87]\n"
"v_pk_add_f32 v[120:121], v[120:121], v[88:89]\n"
"v_pk_add_f32 v[122:123], v[122:123], v[90:91]\n"
"v_pk_add_f32 v[124:125], v[124:125], v[92:93]\n"
"v_pk_add_f32 v[126:127], v[126:127], v[94:95]\n"
"s_waitcnt lgkmcnt(9)\n"
"v_pk_add_f32 v[128:129], v[128:129], v[132:133]\n"
"v_pk_add_f32 v[112:113], v[112:113], v[136:137]\n"
"s_nop 0\n"
"v_pk_add_f32 v[130:131], v[130:131], v[134:135]\n"
"v_pk_add_f32 v[114:115], v[114:115], v[138:139]\n"
"v_pk_add_f32 v[112:113], v[112:113], v[128:129]\n"
"v_pk_add_f32 v[114:115], v[114:115], v[130:131]\n"
"v_cvt_pk_bf16_f32 v80, v112, v113\n"
"v_cvt_pk_bf16_f32 v81, v114, v115\n"
"s_waitcnt lgkmcnt(6)\n"
"v_pk_add_f32 v[140:141], v[140:141], v[144:145]\n"
"s_nop 0\n"
"v_pk_add_f32 v[116:117], v[116:117], v[148:149]\n"
"v_pk_add_f32 v[142:143], v[142:143], v[146:147]\n"
"v_pk_add_f32 v[118:119], v[118:119], v[150:151]\n"
"v_pk_add_f32 v[116:117], v[116:117], v[140:141]\n"
"v_pk_add_f32 v[118:119], v[118:119], v[142:143]\n"
"v_cvt_pk_bf16_f32 v82, v116, v117\n"
"v_cvt_pk_bf16_f32 v83, v118, v119\n"
"buffer_store_dwordx4 v[80:83], v17, s[24:27], s91 offen offset:0\n"
"s_waitcnt lgkmcnt(3)\n"
"v_pk_add_f32 v[152:153], v[152:153], v[156:157]\n"
"v_pk_add_f32 v[120:121], v[120:121], v[160:161]\n"
"v_pk_add_f32 v[154:155], v[154:155], v[158:159]\n"
"s_nop 0\n"
"v_pk_add_f32 v[122:123], v[122:123], v[162:163]\n"
"v_pk_add_f32 v[120:121], v[120:121], v[152:153]\n"
"v_pk_add_f32 v[122:123], v[122:123], v[154:155]\n"
"v_cvt_pk_bf16_f32 v84, v120, v121\n"
"v_cvt_pk_bf16_f32 v85, v122, v123\n"
"s_waitcnt lgkmcnt(0)\n"
"v_pk_add_f32 v[164:165], v[164:165], v[168:169]\n"
"v_pk_add_f32 v[124:125], v[124:125], v[172:173]\n"
"s_nop 0\n"
"v_pk_add_f32 v[166:167], v[166:167], v[170:171]\n"
"v_pk_add_f32 v[126:127], v[126:127], v[174:175]\n"
"v_pk_add_f32 v[124:125], v[124:125], v[164:165]\n"
"v_pk_add_f32 v[126:127], v[126:127], v[166:167]\n"
"v_cvt_pk_bf16_f32 v86, v124, v125\n"
"v_cvt_pk_bf16_f32 v87, v126, v127\n"
"buffer_store_dwordx4 v[84:87], v17, s[24:27], s91 offen offset:32\n"
"s_endpgm\n"
"L_f1:\n"
"s_mov_b32 s66, 65536\n"
"L_f1_loop:\n"
"s_sleep 1\n"
"buffer_load_dwordx2 v[44:45], v126, s[4:7], s83 offen sc1\n"
"s_waitcnt vmcnt(0)\n"
"v_cmp_ne_u64 vcc, s[62:63], v[44:45]\n"
"s_cbranch_vccz L_f1_back\n"
"s_sub_u32 s66, s66, 1\n"
"s_cbranch_scc0 L_f1_loop\n"
"s_branch L_f1_back\n"
"L_fw:\n"
"s_mov_b32 s66, 65536\n"
"L_fw_loop:\n"
"s_sleep 1\n"
"buffer_load_dwordx2 v[44:45], v47, s[4:7], s83 offen sc1\n"
"s_waitcnt vmcnt(0)\n"
"v_cmp_ne_u64 vcc, s[62:63], v[44:45]\n"
"s_cbranch_vccz L_fw_back\n"
"s_sub_u32 s66, s66, 1\n"
"s_cbranch_scc0 L_fw_loop\n"
"s_branch L_fw_back\n"
"L_p3:\n"
"s_mov_b32 s66, 65536\n"
"L_p3_loop:\n"
"s_sleep 1\n"
"buffer_load_dwordx2 v[6:7], v4, s[4:7], s37 offen offset:128 sc1\n"
"s_waitcnt vmcnt(0)\n"
"v_cmp_ne_u64 vcc, s[62:63], v[6:7]\n"
"s_cbranch_vccz L_p3_back\n"
"s_sub_u32 s66, s66, 1\n"
"s_cbranch_scc0 L_p3_loop\n"
"s_branch L_p3_back\n"
"L_hm20:\n"
"s_mov_b32 s66, 65536\n"
"L_hm20_loop:\n"
"s_sleep 1\n"
"buffer_load_dwordx4 v[160:163], v44, s[4:7], s72 offen offset:1024 sc1\n"
"buffer_load_dwordx4 v[164:167], v45, s[4:7], s72 offen offset:1024 sc1\n"
"s_waitcnt vmcnt(0)\n"
"v_max3_u32 v18, v160, v161, v162\n"
"v_max3_u32 v18, v18, v163, v164\n"
"v_max3_u32 v18, v18, v165, v166\n"
"v_max_u32 v18, v18, v167\n"
"v_cmp_eq_u32 vcc, -1, v18\n"
"s_cbranch_vccz L_hm20_back\n"
"s_sub_u32 s66, s66, 1\n"
"s_cbranch_scc0 L_hm20_loop\n"
"s_branch L_hm20_back\n"
"L_hm21:\n"
"s_mov_b32 s66, 65536\n"
"L_hm21_loop:\n"
"s_sleep 1\n"
"buffer_load_dwordx4 v[168:171], v44, s[4:7], s74 offen offset:1024 sc1\n"
"buffer_load_dwordx4 v[172:175], v45, s[4:7], s74 offen offset:1024 sc1\n"
"s_waitcnt vmcnt(0)\n"
"v_max3_u32 v18, v168, v169, v170\n"
"v_max3_u32 v18, v18, v171, v172\n"
"v_max3_u32 v18, v18, v173, v174\n"
"v_max_u32 v18, v18, v175\n"
"v_cmp_eq_u32 vcc, -1, v18\n"
"s_cbranch_vccz L_hm21_back\n"
"s_sub_u32 s66, s66, 1\n"
"s_cbranch_scc0 L_hm21_loop\n"
"s_branch L_hm21_back\n"
"L_hm22:\n"
"s_mov_b32 s66, 65536\n"
"L_hm22_loop:\n"
"s_sleep 1\n"
"buffer_load_dwordx4 v[176:179], v44, s[4:7], s76 offen offset:1024 sc1\n"
"buffer_load_dwordx4 v[180:183], v45, s[4:7], s76 offen offset:1024 sc1\n"
"s_waitcnt vmcnt(0)\n"
"v_max3_u32 v18, v176, v177, v178\n"
"v_max3_u32 v18, v18, v179, v180\n"
"v_max3_u32 v18, v18, v181, v182\n"
"v_max_u32 v18, v18, v183\n"
"v_cmp_eq_u32 vcc, -1, v18\n"
"s_cbranch_vccz L_hm22_back\n"
"s_sub_u32 s66, s66, 1\n"
"s_cbranch_scc0 L_hm22_loop\n"
"s_branch L_hm22_back\n"
"L_hm23:\n"
"s_mov_b32 s66, 65536\n"
"L_hm23_loop:\n"
"s_sleep 1\n"
"buffer_load_dwordx4 v[184:187], v44, s[4:7], s78 offen offset:1024 sc1\n"
"buffer_load_dwordx4 v[188:191], v45, s[4:7], s78 offen offset:1024 sc1\n"
"s_waitcnt vmcnt(0)\n"
"v_max3_u32 v18, v184, v185, v186\n"
"v_max3_u32 v18, v18, v187, v188\n"
"v_max3_u32 v18, v18, v189, v190\n"
"v_max_u32 v18, v18, v191\n"
"v_cmp_eq_u32 vcc, -1, v18\n"
"s_cbranch_vccz L_hm23_back\n"
"s_sub_u32 s66, s66, 1\n"
"s_cbranch_scc0 L_hm23_loop\n"
"s_branch L_hm23_back\n"
"L_hm24:\n"
"s_mov_b32 s66, 65536\n"
"L_hm24_loop:\n"
"s_sleep 1\n"
"buffer_load_dwordx4 v[192:195], v44, s[4:7], s72 offen offset:2048 sc1\n"
"buffer_load_dwordx4 v[196:199], v45, s[4:7], s72 offen offset:2048 sc1\n"
"s_waitcnt vmcnt(0)\n"
"v_max3_u32 v18, v192, v193, v194\n"
"v_max3_u32 v18, v18, v195, v196\n"
"v_max3_u32 v18, v18, v197, v198\n"
"v_max_u32 v18, v18, v199\n"
"v_cmp_eq_u32 vcc, -1, v18\n"
"s_cbranch_vccz L_hm24_back\n"
"s_sub_u32 s66, s66, 1\n"
"s_cbranch_scc0 L_hm24_loop\n"
"s_branch L_hm24_back\n"
"L_hm25:\n"
"s_mov_b32 s66, 65536\n"
"L_hm25_loop:\n"
"s_sleep 1\n"
"buffer_load_dwordx4 v[200:203], v44, s[4:7], s74 offen offset:2048 sc1\n"
"buffer_load_dwordx4 v[204:207], v45, s[4:7], s74 offen offset:2048 sc1\n"
"s_waitcnt vmcnt(0)\n"
"v_max3_u32 v18, v200, v201, v202\n"
"v_max3_u32 v18, v18, v203, v204\n"
"v_max3_u32 v18, v18, v205, v206\n"
"v_max_u32 v18, v18, v207\n"
"v_cmp_eq_u32 vcc, -1, v18\n"
"s_cbranch_vccz L_hm25_back\n"
"s_sub_u32 s66, s66, 1\n"
"s_cbranch_scc0 L_hm25_loop\n"
"s_branch L_hm25_back\n"
"L_hm26:\n"
"s_mov_b32 s66, 65536\n"
"L_hm26_loop:\n"
"s_sleep 1\n"
"buffer_load_dwordx4 v[208:211], v44, s[4:7], s76 offen offset:2048 sc1\n"
"buffer_load_dwordx4 v[212:215], v45, s[4:7], s76 offen offset:2048 sc1\n"
"s_waitcnt vmcnt(0)\n"
"v_max3_u32 v18, v208, v209, v210\n"
"v_max3_u32 v18, v18, v211, v212\n"
"v_max3_u32 v18, v18, v213, v214\n"
"v_max_u32 v18, v18, v215\n"
"v_cmp_eq_u32 vcc, -1, v18\n"
"s_cbranch_vccz L_hm26_back\n"
"s_sub_u32 s66, s66, 1\n"
"s_cbranch_scc0 L_hm26_loop\n"
"s_branch L_hm26_back\n"
"L_hm27:\n"
"s_mov_b32 s66, 65536\n"
"L_hm27_loop:\n"
"s_sleep 1\n"
"buffer_load_dwordx4 v[216:219], v44, s[4:7], s78 offen offset:2048 sc1\n"
"buffer_load_dwordx4 v[220:223], v45, s[4:7], s78 offen offset:2048 sc1\n"
"s_waitcnt vmcnt(0)\n"
"v_max3_u32 v18, v216, v217, v218\n"
"v_max3_u32 v18, v18, v219, v220\n"
"v_max3_u32 v18, v18, v221, v222\n"
"v_max_u32 v18, v18, v223\n"
"v_cmp_eq_u32 vcc, -1, v18\n"
"s_cbranch_vccz L_hm27_back\n"
"s_sub_u32 s66, s66, 1\n"
"s_cbranch_scc0 L_hm27_loop\n"
"s_branch L_hm27_back\n"
"L_redo:\n"
"s_nop 15\n"
"s_mov_b32 s66, 65536\n"
"L_redo0:\n"
"buffer_load_dwordx4 v[128:131], v44, s[4:7], s71 offen offset:0 sc1\n"
"buffer_load_dwordx4 v[132:135], v45, s[4:7], s71 offen offset:0 sc1\n"
"s_waitcnt vmcnt(0)\n"
"v_max3_u32 v18, v128, v129, v130\n"
"v_max3_u32 v18, v18, v131, v132\n"
"v_max3_u32 v18, v18, v133, v134\n"
"v_max_u32 v18, v18, v135\n"
"v_cmp_eq_u32 vcc, -1, v18\n"
"s_cbranch_vccz L_redo0k\n"
"s_sleep 1\n"
"s_sub_u32 s66, s66, 1\n"
"s_cbranch_scc0 L_redo0\n"
"L_redo0k:\n"
"v_mfma_f32_32x32x16_bf16 v[112:127], a[0:3], v[128:131], 0\n"
"v_mfma_f32_32x32x16_bf16 a[224:239], a[0:3], v[132:135], 0\n"
"v_mfma_f32_32x32x16_bf16 a[240:255], a[4:7], v[128:131], 0\n"
"v_mfma_f32_32x32x16_bf16 v[64:79], a[4:7], v[132:135], 0\n"
"s_mov_b32 s66, 65536\n"
"L_redo1:\n"
"buffer_load_dwordx4 v[128:131], v44, s[4:7], s73 offen offset:0 sc1\n"
"buffer_load_dwordx4 v[132:135], v45, s[4:7], s73 offen offset:0 sc1\n"
"s_waitcnt vmcnt(0)\n"
"v_max3_u32 v18, v128, v129, v130\n"
"v_max3_u32 v18, v18, v131, v132\n"
"v_max3_u32 v18, v18, v133, v134\n"
"v_max_u32 v18, v18, v135\n"
"v_cmp_eq_u32_e64 vcc, -1, v18\n"
"s_cbranch_vccz L_redo1k\n"
"s_sleep 1\n"
"s_sub_u32 s66, s66, 1\n"
"s_cbranch_scc0 L_redo1\n"
"L_redo1k:\n"
"v_mfma_f32_32x32x16_bf16 v[112:127], a[8:11], v[128:131], v[112:127]\n"
"v_mfma_f32_32x32x16_bf16 a[224:239], a[8:11], v[132:135], a[224:239]\n"
"v_mfma_f32_32x32x16_bf16 a[240:255], a[12:15], v[128:131], a[240:255]\n"
"v_mfma_f32_32x32x16_bf16 v[64:79], a[12:15], v[132:135], v[64:79]\n"
"s_mov_b32 s66, 65536\n"
"L_redo2:\n"
"buffer_load_dwordx4 v[128:131], v44, s[4:7], s75 offen offset:0 sc1\n"
"buffer_load_dwordx4 v[132:135], v45, s[4:7], s75 offen offset:0 sc1\n"
"s_waitcnt vmcnt(0)\n"
"v_max3_u32 v18, v128, v129, v130\n"
"v_max3_u32 v18, v18, v131, v132\n"
"v_max3_u32 v18, v18, v133, v134\n"
"v_max_u32 v18, v18, v135\n"
"v_cmp_eq_u32 vcc, -1, v18\n"
"s_cbranch_vccz L_redo2k\n"
"s_sleep 1\n"
"s_sub_u32 s66, s66, 1\n"
"s_cbranch_scc0 L_redo2\n"
"L_redo2k:\n"
"v_mfma_f32_32x32x16_bf16 v[112:127], a[16:19], v[128:131], v[112:127]\n"
"v_mfma_f32_32x32x16_bf16 a[224:239], a[16:19], v[132:135], a[224:239]\n"
"s_nop 0\n"
"v_mfma_f32_32x32x16_bf16 a[240:255], a[20:23], v[128:131], a[240:255]\n"
"v_mfma_f32_32x32x16_bf16 v[64:79], a[20:23], v[132:135], v[64:79]\n"
"s_mov_b32 s66, 65536\n"
"L_redo3:\n"
"buffer_load_dwordx4 v[128:131], v44, s[4:7], s77 offen offset:0 sc1\n"
"buffer_load_dwordx4 v[132:135], v45, s[4:7], s77 offen offset:0 sc1\n"
"s_waitcnt vmcnt(0)\n"
"v_max3_u32 v18, v128, v129, v130\n"
"v_max3_u32 v18, v18, v131, v132\n"
"v_max3_u32 v18, v18, v133, v134\n"
"v_max_u32 v18, v18, v135\n"
"v_cmp_eq_u32_e64 vcc, -1, v18\n"
"s_cbranch_vccz L_redo3k\n"
"s_sleep 1\n"
"s_sub_u32 s66, s66, 1\n"
"s_cbranch_scc0 L_redo3\n"
"L_redo3k:\n"
"v_mfma_f32_32x32x16_bf16 v[112:127], a[24:27], v[128:131], v[112:127]\n"
"v_mfma_f32_32x32x16_bf16 a[224:239], a[24:27], v[132:135], a[224:239]\n"
"v_mfma_f32_32x32x16_bf16 a[240:255], a[28:31], v[128:131], a[240:255]\n"
"v_mfma_f32_32x32x16_bf16 v[64:79], a[28:31], v[132:135], v[64:79]\n"
"s_mov_b32 s66, 65536\n"
"L_redo4:\n"
"buffer_load_dwordx4 v[128:131], v44, s[4:7], s71 offen offset:1024 sc1\n"
"buffer_load_dwordx4 v[132:135], v45, s[4:7], s71 offen offset:1024 sc1\n"
"s_waitcnt vmcnt(0)\n"
"v_max3_u32 v18, v128, v129, v130\n"
"v_max3_u32 v18, v18, v131, v132\n"
"v_max3_u32 v18, v18, v133, v134\n"
"v_max_u32 v18, v18, v135\n"
"v_cmp_eq_u32 vcc, -1, v18\n"
"s_cbranch_vccz L_redo4k\n"
"s_sleep 1\n"
"s_sub_u32 s66, s66, 1\n"
"s_cbranch_scc0 L_redo4\n"
"L_redo4k:\n"
"v_mfma_f32_32x32x16_bf16 v[112:127], a[32:35], v[128:131], v[112:127]\n"
"v_mfma_f32_32x32x16_bf16 a[224:239], a[32:35], v[132:135], a[224:239]\n"
"s_nop 0\n"
"v_mfma_f32_32x32x16_bf16 a[240:255], a[36:39], v[128:131], a[240:255]\n"
"v_mfma_f32_32x32x16_bf16 v[64:79], a[36:39], v[132:135], v[64:79]\n"
"s_mov_b32 s66, 65536\n"
"L_redo5:\n"
"buffer_load_dwordx4 v[128:131], v44, s[4:7], s73 offen offset:1024 sc1\n"
"buffer_load_dwordx4 v[132:135], v45, s[4:7], s73 offen offset:1024 sc1\n"
"s_waitcnt vmcnt(0)\n"
"v_max3_u32 v18, v128, v129, v130\n"
"v_max3_u32 v18, v18, v131, v132\n"
"v_max3_u32 v18, v18, v133, v134\n"
"v_max_u32 v18, v18, v135\n"
"v_cmp_eq_u32_e64 vcc, -1, v18\n"
"s_cbranch_vccz L_redo5k\n"
"s_sleep 1\n"
"s_sub_u32 s66, s66, 1\n"
"s_cbranch_scc0 L_redo5\n"
"L_redo5k:\n"
"v_mfma_f32_32x32x16_bf16 v[112:127], a[40:43], v[128:131], v[112:127]\n"
"v_mfma_f32_32x32x16_bf16 a[224:239], a[40:43], v[132:135], a[224:239]\n"
"v_mfma_f32_32x32x16_bf16 a[240:255], a[44:47], v[128:131], a[240:255]\n"
"v_mfma_f32_32x32x16_bf16 v[64:79], a[44:47], v[132:135], v[64:79]\n"
"s_mov_b32 s66, 65536\n"
"L_redo6:\n"
"buffer_load_dwordx4 v[128:131], v44, s[4:7], s75 offen offset:1024 sc1\n"
"buffer_load_dwordx4 v[132:135], v45, s[4:7], s75 offen offset:1024 sc1\n"
"s_waitcnt vmcnt(0)\n"
"v_max3_u32 v18, v128, v129, v130\n"
"v_max3_u32 v18, v18, v131, v132\n"
"v_max3_u32 v18, v18, v133, v134\n"
"v_max_u32 v18, v18, v135\n"
"v_cmp_eq_u32 vcc, -1, v18\n"
"s_cbranch_vccz L_redo6k\n"
"s_sleep 1\n"
"s_sub_u32 s66, s66, 1\n"
"s_cbranch_scc0 L_redo6\n"
"L_redo6k:\n"
"v_mfma_f32_32x32x16_bf16 v[112:127], a[48:51], v[128:131], v[112:127]\n"
"v_mfma_f32_32x32x16_bf16 a[224:239], a[48:51], v[132:135], a[224:239]\n"
"s_nop 0\n"
"v_mfma_f32_32x32x16_bf16 a[240:255], a[52:55], v[128:131], a[240:255]\n"
"v_mfma_f32_32x32x16_bf16 v[64:79], a[52:55], v[132:135], v[64:79]\n"
"s_mov_b32 s66, 65536\n"
"L_redo7:\n"
"buffer_load_dwordx4 v[128:131], v44, s[4:7], s77 offen offset:1024 sc1\n"
"buffer_load_dwordx4 v[132:135], v45, s[4:7], s77 offen offset:1024 sc1\n"
"s_waitcnt vmcnt(0)\n"
"v_max3_u32 v18, v128, v129, v130\n"
"v_max3_u32 v18, v18, v131, v132\n"
"v_max3_u32 v18, v18, v133, v134\n"
"v_max_u32 v18, v18, v135\n"
"v_cmp_eq_u32_e64 vcc, -1, v18\n"
"s_cbranch_vccz L_redo7k\n"
"s_sleep 1\n"
"s_sub_u32 s66, s66, 1\n"
"s_cbranch_scc0 L_redo7\n"
"L_redo7k:\n"
"v_mfma_f32_32x32x16_bf16 v[112:127], a[56:59], v[128:131], v[112:127]\n"
"v_mfma_f32_32x32x16_bf16 a[224:239], a[56:59], v[132:135], a[224:239]\n"
"v_mfma_f32_32x32x16_bf16 a[240:255], a[60:63], v[128:131], a[240:255]\n"
"v_mfma_f32_32x32x16_bf16 v[64:79], a[60:63], v[132:135], v[64:79]\n"
"s_mov_b32 s66, 65536\n"
"L_redo8:\n"
"buffer_load_dwordx4 v[128:131], v44, s[4:7], s71 offen offset:2048 sc1\n"
"buffer_load_dwordx4 v[132:135], v45, s[4:7], s71 offen offset:2048 sc1\n"
"s_waitcnt vmcnt(0)\n"
"v_max3_u32 v18, v128, v129, v130\n"
"v_max3_u32 v18, v18, v131, v132\n"
"v_max3_u32 v18, v18, v133, v134\n"
"v_max_u32 v18, v18, v135\n"
"v_cmp_eq_u32 vcc, -1, v18\n"
"s_cbranch_vccz L_redo8k\n"
"s_sleep 1\n"
"s_sub_u32 s66, s66, 1\n"
"s_cbranch_scc0 L_redo8\n"
"L_redo8k:\n"
"v_mfma_f32_32x32x16_bf16 v[112:127], a[64:67], v[128:131], v[112:127]\n"
"v_mfma_f32_32x32x16_bf16 a[224:239], a[64:67], v[132:135], a[224:239]\n"
"s_nop 0\n"
"v_mfma_f32_32x32x16_bf16 a[240:255], a[68:71], v[128:131], a[240:255]\n"
"v_mfma_f32_32x32x16_bf16 v[64:79], a[68:71], v[132:135], v[64:79]\n"
"s_mov_b32 s66, 65536\n"
"L_redo9:\n"
"buffer_load_dwordx4 v[128:131], v44, s[4:7], s73 offen offset:2048 sc1\n"
"buffer_load_dwordx4 v[132:135], v45, s[4:7], s73 offen offset:2048 sc1\n"
"s_waitcnt vmcnt(0)\n"
"v_max3_u32 v18, v128, v129, v130\n"
"v_max3_u32 v18, v18, v131, v132\n"
"v_max3_u32 v18, v18, v133, v134\n"
"v_max_u32 v18, v18, v135\n"
"v_cmp_eq_u32_e64 vcc, -1, v18\n"
"s_cbranch_vccz L_redo9k\n"
"s_sleep 1\n"
"s_sub_u32 s66, s66, 1\n"
"s_cbranch_scc0 L_redo9\n"
"L_redo9k:\n"
"v_mfma_f32_32x32x16_bf16 v[112:127], a[72:75], v[128:131], v[112:127]\n"
"v_mfma_f32_32x32x16_bf16 a[224:239], a[72:75], v[132:135], a[224:239]\n"
"v_mfma_f32_32x32x16_bf16 a[240:255], a[76:79], v[128:131], a[240:255]\n"
"v_mfma_f32_32x32x16_bf16 v[64:79], a[76:79], v[132:135], v[64:79]\n"
"s_mov_b32 s66, 65536\n"
"L_redo10:\n"
"buffer_load_dwordx4 v[128:131], v44, s[4:7], s75 offen offset:2048 sc1\n"
"buffer_load_dwordx4 v[132:135], v45, s[4:7], s75 offen offset:2048 sc1\n"
"s_waitcnt vmcnt(0)\n"
"v_max3_u32 v18, v128, v129, v130\n"
"v_max3_u32 v18, v18, v131, v132\n"
"v_max3_u32 v18, v18, v133, v134\n"
"v_max_u32 v18, v18, v135\n"
"v_cmp_eq_u32 vcc, -1, v18\n"
"s_cbranch_vccz L_redo10k\n"
"s_sleep 1\n"
"s_sub_u32 s66, s66, 1\n"
"s_cbranch_scc0 L_redo10\n"
"L_redo10k:\n"
"v_mfma_f32_32x32x16_bf16 v[112:127], a[80:83], v[128:131], v[112:127]\n"
"v_mfma_f32_32x32x16_bf16 a[224:239], a[80:83], v[132:135], a[224:239]\n"
"s_nop 0\n"
"v_mfma_f32_32x32x16_bf16 a[240:255], a[84:87], v[128:131], a[240:255]\n"
"v_mfma_f32_32x32x16_bf16 v[64:79], a[84:87], v[132:135], v[64:79]\n"
"s_mov_b32 s66, 65536\n"
"L_redo11:\n"
"buffer_load_dwordx4 v[128:131], v44, s[4:7], s77 offen offset:2048 sc1\n"
"buffer_load_dwordx4 v[132:135], v45, s[4:7], s77 offen offset:2048 sc1\n"
"s_waitcnt vmcnt(0)\n"
"v_max3_u32 v18, v128, v129, v130\n"
"v_max3_u32 v18, v18, v131, v132\n"
"v_max3_u32 v18, v18, v133, v134\n"
"v_max_u32 v18, v18, v135\n"
"v_cmp_eq_u32_e64 vcc, -1, v18\n"
"s_cbranch_vccz L_redo11k\n"
"s_sleep 1\n"
"s_sub_u32 s66, s66, 1\n"
"s_cbranch_scc0 L_redo11\n"
"L_redo11k:\n"
"v_mfma_f32_32x32x16_bf16 v[112:127], a[88:91], v[128:131], v[112:127]\n"
"v_mfma_f32_32x32x16_bf16 a[224:239], a[88:91], v[132:135], a[224:239]\n"
"v_mfma_f32_32x32x16_bf16 a[240:255], a[92:95], v[128:131], a[240:255]\n"
"v_mfma_f32_32x32x16_bf16 v[64:79], a[92:95], v[132:135], v[64:79]\n"
"s_mov_b32 s66, 65536\n"
"L_redo12:\n"
"buffer_load_dwordx4 v[128:131], v44, s[4:7], s71 offen offset:3072 sc1\n"
"buffer_load_dwordx4 v[132:135], v45, s[4:7], s71 offen offset:3072 sc1\n"
"s_waitcnt vmcnt(0)\n"
"v_max3_u32 v18, v128, v129, v130\n"
"v_max3_u32 v18, v18, v131, v132\n"
"v_max3_u32 v18, v18, v133, v134\n"
"v_max_u32 v18, v18, v135\n"
"v_cmp_eq_u32 vcc, -1, v18\n"
"s_cbranch_vccz L_redo12k\n"
"s_sleep 1\n"
"s_sub_u32 s66, s66, 1\n"
"s_cbranch_scc0 L_redo12\n"
"L_redo12k:\n"
"v_mfma_f32_32x32x16_bf16 v[112:127], a[96:99], v[128:131], v[112:127]\n"
"v_mfma_f32_32x32x16_bf16 a[224:239], a[96:99], v[132:135], a[224:239]\n"
"s_nop 0\n"
"v_mfma_f32_32x32x16_bf16 a[240:255], a[100:103], v[128:131], a[240:255]\n"
"v_mfma_f32_32x32x16_bf16 v[64:79], a[100:103], v[132:135], v[64:79]\n"
"s_mov_b32 s66, 65536\n"
"L_redo13:\n"
"buffer_load_dwordx4 v[128:131], v44, s[4:7], s73 offen offset:3072 sc1\n"
"buffer_load_dwordx4 v[132:135], v45, s[4:7], s73 offen offset:3072 sc1\n"
"s_waitcnt vmcnt(0)\n"
"v_max3_u32 v18, v128, v129, v130\n"
"v_max3_u32 v18, v18, v131, v132\n"
"v_max3_u32 v18, v18, v133, v134\n"
"v_max_u32 v18, v18, v135\n"
"v_cmp_eq_u32_e64 vcc, -1, v18\n"
"s_cbranch_vccz L_redo13k\n"
"s_sleep 1\n"
"s_sub_u32 s66, s66, 1\n"
"s_cbranch_scc0 L_redo13\n"
"L_redo13k:\n"
"v_mfma_f32_32x32x16_bf16 v[112:127], a[104:107], v[128:131], v[112:127]\n"
"v_mfma_f32_32x32x16_bf16 a[224:239], a[104:107], v[132:135], a[224:239]\n"
"v_mfma_f32_32x32x16_bf16 a[240:255], a[108:111], v[128:131], a[240:255]\n"
"v_mfma_f32_32x32x16_bf16 v[64:79], a[108:111], v[132:135], v[64:79]\n"
"s_mov_b32 s66, 65536\n"
"L_redo14:\n"
"buffer_load_dwordx4 v[128:131], v44, s[4:7], s75 offen offset:3072 sc1\n"
"buffer_load_dwordx4 v[132:135], v45, s[4:7], s75 offen offset:3072 sc1\n"
"s_waitcnt vmcnt(0)\n"
"v_max3_u32 v18, v128, v129, v130\n"
"v_max3_u32 v18, v18, v131, v132\n"
"v_max3_u32 v18, v18, v133, v134\n"
"v_max_u32 v18, v18, v135\n"
"v_cmp_eq_u32 vcc, -1, v18\n"
"s_cbranch_vccz L_redo14k\n"
"s_sleep 1\n"
"s_sub_u32 s66, s66, 1\n"
"s_cbranch_scc0 L_redo14\n"
"L_redo14k:\n"
"v_mfma_f32_32x32x16_bf16 v[112:127], a[112:115], v[128:131], v[112:127]\n"
"v_mfma_f32_32x32x16_bf16 a[224:239], a[112:115], v[132:135], a[224:239]\n"
"s_nop 0\n"
"v_mfma_f32_32x32x16_bf16 a[240:255], a[116:119], v[128:131], a[240:255]\n"
"v_mfma_f32_32x32x16_bf16 v[64:79], a[116:119], v[132:135], v[64:79]\n"
"s_mov_b32 s66, 65536\n"
"L_redo15:\n"
"buffer_load_dwordx4 v[128:131], v44, s[4:7], s77 offen offset:3072 sc1\n"
"buffer_load_dwordx4 v[132:135], v45, s[4:7], s77 offen offset:3072 sc1\n"
"s_waitcnt vmcnt(0)\n"
"v_max3_u32 v18, v128, v129, v130\n"
"v_max3_u32 v18, v18, v131, v132\n"
"v_max3_u32 v18, v18, v133, v134\n"
"v_max_u32 v18, v18, v135\n"
"v_cmp_eq_u32_e64 vcc, -1, v18\n"
"s_cbranch_vccz L_redo15k\n"
"s_sleep 1\n"
"s_sub_u32 s66, s66, 1\n"
"s_cbranch_scc0 L_redo15\n"
"L_redo15k:\n"
"v_mfma_f32_32x32x16_bf16 v[112:127], a[120:123], v[128:131], v[112:127]\n"
"v_mfma_f32_32x32x16_bf16 a[224:239], a[120:123], v[132:135], a[224:239]\n"
"v_mfma_f32_32x32x16_bf16 a[240:255], a[124:127], v[128:131], a[240:255]\n"
"v_mfma_f32_32x32x16_bf16 v[64:79], a[124:127], v[132:135], v[64:79]\n"
"s_mov_b32 s66, 65536\n"
"L_redo16:\n"
"buffer_load_dwordx4 v[128:131], v44, s[4:7], s72 offen offset:0 sc1\n"
"buffer_load_dwordx4 v[132:135], v45, s[4:7], s72 offen offset:0 sc1\n"
"s_waitcnt vmcnt(0)\n"
"v_max3_u32 v18, v128, v129, v130\n"
"v_max3_u32 v18, v18, v131, v132\n"
"v_max3_u32 v18, v18, v133, v134\n"
"v_max_u32 v18, v18, v135\n"
"v_cmp_eq_u32 vcc, -1, v18\n"
"s_cbranch_vccz L_redo16k\n"
"s_sleep 1\n"
"s_sub_u32 s66, s66, 1\n"
"s_cbranch_scc0 L_redo16\n"
"L_redo16k:\n"
"v_mfma_f32_32x32x16_bf16 v[112:127], a[128:131], v[128:131], v[112:127]\n"
"v_mfma_f32_32x32x16_bf16 a[224:239], a[128:131], v[132:135], a[224:239]\n"
"s_nop 0\n"
"v_mfma_f32_32x32x16_bf16 a[240:255], a[132:135], v[128:131], a[240:255]\n"
"v_mfma_f32_32x32x16_bf16 v[64:79], a[132:135], v[132:135], v[64:79]\n"
"s_mov_b32 s66, 65536\n"
"L_redo17:\n"
"buffer_load_dwordx4 v[128:131], v44, s[4:7], s74 offen offset:0 sc1\n"
"buffer_load_dwordx4 v[132:135], v45, s[4:7], s74 offen offset:0 sc1\n"
"s_waitcnt vmcnt(0)\n"
"v_max3_u32 v18, v128, v129, v130\n"
"v_max3_u32 v18, v18, v131, v132\n"
"v_max3_u32 v18, v18, v133, v134\n"
"v_max_u32 v18, v18, v135\n"
"v_cmp_eq_u32_e64 vcc, -1, v18\n"
"s_cbranch_vccz L_redo17k\n"
"s_sleep 1\n"
"s_sub_u32 s66, s66, 1\n"
"s_cbranch_scc0 L_redo17\n"
"L_redo17k:\n"
"v_mfma_f32_32x32x16_bf16 v[112:127], a[136:139], v[128:131], v[112:127]\n"
"v_mfma_f32_32x32x16_bf16 a[224:239], a[136:139], v[132:135], a[224:239]\n"
"v_mfma_f32_32x32x16_bf16 a[240:255], a[140:143], v[128:131], a[240:255]\n"
"v_mfma_f32_32x32x16_bf16 v[64:79], a[140:143], v[132:135], v[64:79]\n"
"s_mov_b32 s66, 65536\n"
"L_redo18:\n"
"buffer_load_dwordx4 v[128:131], v44, s[4:7], s76 offen offset:0 sc1\n"
"buffer_load_dwordx4 v[132:135], v45, s[4:7], s76 offen offset:0 sc1\n"
"s_waitcnt vmcnt(0)\n"
"v_max3_u32 v18, v128, v129, v130\n"
"v_max3_u32 v18, v18, v131, v132\n"
"v_max3_u32 v18, v18, v133, v134\n"
"v_max_u32 v18, v18, v135\n"
"v_cmp_eq_u32 vcc, -1, v18\n"
"s_cbranch_vccz L_redo18k\n"
"s_sleep 1\n"
"s_sub_u32 s66, s66, 1\n"
"s_cbranch_scc0 L_redo18\n"
"L_redo18k:\n"
"v_mfma_f32_32x32x16_bf16 v[112:127], a[144:147], v[128:131], v[112:127]\n"
"v_mfma_f32_32x32x16_bf16 a[224:239], a[144:147], v[132:135], a[224:239]\n"
"s_nop 0\n"
"v_mfma_f32_32x32x16_bf16 a[240:255], a[148:151], v[128:131], a[240:255]\n"
"v_mfma_f32_32x32x16_bf16 v[64:79], a[148:151], v[132:135], v[64:79]\n"
"s_mov_b32 s66, 65536\n"
"L_redo19:\n"
"buffer_load_dwordx4 v[128:131], v44, s[4:7], s78 offen offset:0 sc1\n"
"buffer_load_dwordx4 v[132:135], v45, s[4:7], s78 offen offset:0 sc1\n"
"s_waitcnt vmcnt(0)\n"
"v_max3_u32 v18, v128, v129, v130\n"
"v_max3_u32 v18, v18, v131, v132\n"
"v_max3_u32 v18, v18, v133, v134\n"
"v_max_u32 v18, v18, v135\n"
"v_cmp_eq_u32_e64 vcc, -1, v18\n"
"s_cbranch_vccz L_redo19k\n"
"s_sleep 1\n"
"s_sub_u32 s66, s66, 1\n"
"s_cbranch_scc0 L_redo19\n"
"L_redo19k:\n"
"v_mfma_f32_32x32x16_bf16 v[112:127], a[152:155], v[128:131], v[112:127]\n"
"v_mfma_f32_32x32x16_bf16 a[224:239], a[152:155], v[132:135], a[224:239]\n"
"v_mfma_f32_32x32x16_bf16 a[240:255], a[156:159], v[128:131], a[240:255]\n"
"v_mfma_f32_32x32x16_bf16 v[64:79], a[156:159], v[132:135], v[64:79]\n"
"s_mov_b32 s66, 65536\n"
"L_redo20:\n"
"buffer_load_dwordx4 v[128:131], v44, s[4:7], s72 offen offset:1024 sc1\n"
"buffer_load_dwordx4 v[132:135], v45, s[4:7], s72 offen offset:1024 sc1\n"
"s_waitcnt vmcnt(0)\n"
"v_max3_u32 v18, v128, v129, v130\n"
"v_max3_u32 v18, v18, v131, v132\n"
"v_max3_u32 v18, v18, v133, v134\n"
"v_max_u32 v18, v18, v135\n"
"v_cmp_eq_u32 vcc, -1, v18\n"
"s_cbranch_vccz L_redo20k\n"
"s_sleep 1\n"
"s_sub_u32 s66, s66, 1\n"
"s_cbranch_scc0 L_redo20\n"
"L_redo20k:\n"
"v_mfma_f32_32x32x16_bf16 v[112:127], a[160:163], v[128:131], v[112:127]\n"
"v_mfma_f32_32x32x16_bf16 a[224:239], a[160:163], v[132:135], a[224:239]\n"
"s_nop 0\n"
"v_mfma_f32_32x32x16_bf16 a[240:255], a[164:167], v[128:131], a[240:255]\n"
"v_mfma_f32_32x32x16_bf16 v[64:79], a[164:167], v[132:135], v[64:79]\n"
"s_mov_b32 s66, 65536\n"
"L_redo21:\n"
"buffer_load_dwordx4 v[128:131], v44, s[4:7], s74 offen offset:1024 sc1\n"
"buffer_load_dwordx4 v[132:135], v45, s[4:7], s74 offen offset:1024 sc1\n"
"s_waitcnt vmcnt(0)\n"
"v_max3_u32 v18, v128, v129, v130\n"
"v_max3_u32 v18, v18, v131, v132\n"
"v_max3_u32 v18, v18, v133, v134\n"
"v_max_u32 v18, v18, v135\n"
"v_cmp_eq_u32_e64 vcc, -1, v18\n"
"s_cbranch_vccz L_redo21k\n"
"s_sleep 1\n"
"s_sub_u32 s66, s66, 1\n"
"s_cbranch_scc0 L_redo21\n"
"L_redo21k:\n"
"v_mfma_f32_32x32x16_bf16 v[112:127], a[168:171], v[128:131], v[112:127]\n"
"v_mfma_f32_32x32x16_bf16 a[224:239], a[168:171], v[132:135], a[224:239]\n"
"v_mfma_f32_32x32x16_bf16 a[240:255], a[172:175], v[128:131], a[240:255]\n"
"v_mfma_f32_32x32x16_bf16 v[64:79], a[172:175], v[132:135], v[64:79]\n"
"s_mov_b32 s66, 65536\n"
"L_redo22:\n"
"buffer_load_dwordx4 v[128:131], v44, s[4:7], s76 offen offset:1024 sc1\n"
"buffer_load_dwordx4 v[132:135], v45, s[4:7], s76 offen offset:1024 sc1\n"
"s_waitcnt vmcnt(0)\n"
"v_max3_u32 v18, v128, v129, v130\n"
"v_max3_u32 v18, v18, v131, v132\n"
"v_max3_u32 v18, v18, v133, v134\n"
"v_max_u32 v18, v18, v135\n"
"v_cmp_eq_u32 vcc, -1, v18\n"
"s_cbranch_vccz L_redo22k\n"
"s_sleep 1\n"
"s_sub_u32 s66, s66, 1\n"
"s_cbranch_scc0 L_redo22\n"
"L_redo22k:\n"
"v_mfma_f32_32x32x16_bf16 v[112:127], a[176:179], v[128:131], v[112:127]\n"
"v_mfma_f32_32x32x16_bf16 a[224:239], a[176:179], v[132:135], a[224:239]\n"
"s_nop 0\n"
"v_mfma_f32_32x32x16_bf16 a[240:255], a[180:183], v[128:131], a[240:255]\n"
"v_mfma_f32_32x32x16_bf16 v[64:79], a[180:183], v[132:135], v[64:79]\n"
"s_mov_b32 s66, 65536\n"
"L_redo23:\n"
"buffer_load_dwordx4 v[128:131], v44, s[4:7], s78 offen offset:1024 sc1\n"
"buffer_load_dwordx4 v[132:135], v45, s[4:7], s78 offen offset:1024 sc1\n"
"s_waitcnt vmcnt(0)\n"
"v_max3_u32 v18, v128, v129, v130\n"
"v_max3_u32 v18, v18, v131, v132\n"
"v_max3_u32 v18, v18, v133, v134\n"
"v_max_u32 v18, v18, v135\n"
"v_cmp_eq_u32_e64 vcc, -1, v18\n"
"s_cbranch_vccz L_redo23k\n"
"s_sleep 1\n"
"s_sub_u32 s66, s66, 1\n"
"s_cbranch_scc0 L_redo23\n"
"L_redo23k:\n"
"v_mfma_f32_32x32x16_bf16 v[112:127], a[184:187], v[128:131], v[112:127]\n"
"v_mfma_f32_32x32x16_bf16 a[224:239], a[184:187], v[132:135], a[224:239]\n"
"v_mfma_f32_32x32x16_bf16 a[240:255], a[188:191], v[128:131], a[240:255]\n"
"v_mfma_f32_32x32x16_bf16 v[64:79], a[188:191], v[132:135], v[64:79]\n"
"s_mov_b32 s66, 65536\n"
"L_redo24:\n"
"buffer_load_dwordx4 v[128:131], v44, s[4:7], s72 offen offset:2048 sc1\n"
"buffer_load_dwordx4 v[132:135], v45, s[4:7], s72 offen offset:2048 sc1\n"
"s_waitcnt vmcnt(0)\n"
"v_max3_u32 v18, v128, v129, v130\n"
"v_max3_u32 v18, v18, v131, v132\n"
"v_max3_u32 v18, v18, v133, v134\n"
"v_max_u32 v18, v18, v135\n"
"v_cmp_eq_u32 vcc, -1, v18\n"
"s_cbranch_vccz L_redo24k\n"
"s_sleep 1\n"
"s_sub_u32 s66, s66, 1\n"
"s_cbranch_scc0 L_redo24\n"
"L_redo24k:\n"
"v_mfma_f32_32x32x16_bf16 v[112:127], a[192:195], v[128:131], v[112:127]\n"
"v_mfma_f32_32x32x16_bf16 a[224:239], a[192:195], v[132:135], a[224:239]\n"
"s_nop 0\n"
"v_mfma_f32_32x32x16_bf16 a[240:255], a[196:199], v[128:131], a[240:255]\n"
"v_mfma_f32_32x32x16_bf16 v[64:79], a[196:199], v[132:135], v[64:79]\n"
"s_mov_b32 s66, 65536\n"
"L_redo25:\n"
"buffer_load_dwordx4 v[128:131], v44, s[4:7], s74 offen offset:2048 sc1\n"
"buffer_load_dwordx4 v[132:135], v45, s[4:7], s74 offen offset:2048 sc1\n"
"s_waitcnt vmcnt(0)\n"
"v_max3_u32 v18, v128, v129, v130\n"
"v_max3_u32 v18, v18, v131, v132\n"
"v_max3_u32 v18, v18, v133, v134\n"
"v_max_u32 v18, v18, v135\n"
"v_cmp_eq_u32_e64 vcc, -1, v18\n"
"s_cbranch_vccz L_redo25k\n"
"s_sleep 1\n"
"s_sub_u32 s66, s66, 1\n"
"s_cbranch_scc0 L_redo25\n"
"L_redo25k:\n"
"v_mfma_f32_32x32x16_bf16 v[112:127], a[200:203], v[128:131], v[112:127]\n"
"v_mfma_f32_32x32x16_bf16 a[224:239], a[200:203], v[132:135], a[224:239]\n"
"v_mfma_f32_32x32x16_bf16 a[240:255], a[204:207], v[128:131], a[240:255]\n"
"v_mfma_f32_32x32x16_bf16 v[64:79], a[204:207], v[132:135], v[64:79]\n"
"s_mov_b32 s66, 65536\n"
"L_redo26:\n"
"buffer_load_dwordx4 v[128:131], v44, s[4:7], s76 offen offset:2048 sc1\n"
"buffer_load_dwordx4 v[132:135], v45, s[4:7], s76 offen offset:2048 sc1\n"
"s_waitcnt vmcnt(0)\n"
"v_max3_u32 v18, v128, v129, v130\n"
"v_max3_u32 v18, v18, v131, v132\n"
"v_max3_u32 v18, v18, v133, v134\n"
"v_max_u32 v18, v18, v135\n"
"v_cmp_eq_u32 vcc, -1, v18\n"
"s_cbranch_vccz L_redo26k\n"
"s_sleep 1\n"
"s_sub_u32 s66, s66, 1\n"
"s_cbranch_scc0 L_redo26\n"
"L_redo26k:\n"
"v_mfma_f32_32x32x16_bf16 v[112:127], a[208:211], v[128:131], v[112:127]\n"
"v_mfma_f32_32x32x16_bf16 a[224:239], a[208:211], v[132:135], a[224:239]\n"
"s_nop 0\n"
"v_mfma_f32_32x32x16_bf16 a[240:255], a[212:215], v[128:131], a[240:255]\n"
"v_mfma_f32_32x32x16_bf16 v[64:79], a[212:215], v[132:135], v[64:79]\n"
"s_mov_b32 s66, 65536\n"
"L_redo27:\n"
"buffer_load_dwordx4 v[128:131], v44, s[4:7], s78 offen offset:2048 sc1\n"
"buffer_load_dwordx4 v[132:135], v45, s[4:7], s78 offen offset:2048 sc1\n"
"s_waitcnt vmcnt(0)\n"
"v_max3_u32 v18, v128, v129, v130\n"
"v_max3_u32 v18, v18, v131, v132\n"
"v_max3_u32 v18, v18, v133, v134\n"
"v_max_u32 v18, v18, v135\n"
"v_cmp_eq_u32_e64 vcc, -1, v18\n"
"s_cbranch_vccz L_redo27k\n"
"s_sleep 1\n"
"s_sub_u32 s66, s66, 1\n"
"s_cbranch_scc0 L_redo27\n"
"L_redo27k:\n"
"v_mfma_f32_32x32x16_bf16 v[112:127], a[216:219], v[128:131], v[112:127]\n"
"v_mfma_f32_32x32x16_bf16 a[224:239], a[216:219], v[132:135], a[224:239]\n"
"v_mfma_f32_32x32x16_bf16 a[240:255], a[220:223], v[128:131], a[240:255]\n"
"v_mfma_f32_32x32x16_bf16 v[64:79], a[220:223], v[132:135], v[64:79]\n"
"s_nop 15\n"
"ds_write_b128 v20, a[224:227] offset:0\n"
"ds_write_b128 v20, a[228:231] offset:1024\n"
"ds_write_b128 v20, a[232:235] offset:2048\n"
"s_nop 0\n"
"ds_write_b128 v20, a[236:239] offset:3072\n"
"ds_write_b128 v21, a[240:243] offset:0\n"
"ds_write_b128 v21, a[244:247] offset:1024\n"
"ds_write_b128 v21, a[248:251] offset:2048\n"
"ds_write_b128 v21, a[252:255] offset:3072\n"
"ds_write_b128 v22, v[64:67] offset:0\n"
"ds_write_b128 v22, v[68:71] offset:1024\n"
"ds_write_b128 v22, v[72:75] offset:2048\n"
"ds_write_b128 v22, v[76:79] offset:3072\n"
"s_branch L_redo_back\n"
  :: "{s[0:1]}"(__builtin_amdgcn_kernarg_segment_ptr()), "{s2}"(blockIdx.x), "{v0}"(threadIdx.x) : "v1", "v2", "v3", "v4", "v5", "v6", "v7", "v8", "v9", "v10", "v11", "v12", "v13", "v14", "v15", "v16", "v17", "v18", "v19", "v20", "v21", "v22", "v23", "v24", "v25", "v26", "v27", "v28", "v29", "v30", "v31", "v32", "v33", "v34", "v35", "v36", "v37", "v38", "v39", "v40", "v41", "v42", "v43", "v44", "v45", "v46", "v47", "v48", "v49", "v50", "v51", "v52", "v53", "v54", "v55", "v56", "v57", "v58", "v59", "v60", "v61", "v62", "v63", "v64", "v65", "v66", "v67", "v68", "v69", "v70", "v71", "v72", "v73", "v74", "v75", "v76", "v77", "v78", "v79", "v80", "v81", "v82", "v83", "v84", "v85", "v86", "v87", "v88", "v89", "v90", "v91", "v92", "v93", "v94", "v95", "v96", "v97", "v98", "v99", "v100", "v101", "v102", "v103", "v104", "v105", "v106", "v107", "v108", "v109", "v110", "v111", "v112", "v113", "v114", "v115", "v116", "v117", "v118", "v119", "v120", "v121", "v122", "v123", "v124", "v125", "v126", "v127", "v128", "v129", "v130", "v131", "v132", "v133", "v134", "v135", "v136", "v137", "v138", "v139", "v140", "v141", "v142", "v143", "v144", "v145", "v146", "v147", "v148", "v149", "v150", "v151", "v152", "v153", "v154", "v155", "v156", "v157", "v158", "v159", "v160", "v161", "v162", "v163", "v164", "v165", "v166", "v167", "v168", "v169", "v170", "v171", "v172", "v173", "v174", "v175", "v176", "v177", "v178", "v179", "v180", "v181", "v182", "v183", "v184", "v185", "v186", "v187", "v188", "v189", "v190", "v191", "v192", "v193", "v194", "v195", "v196", "v197", "v198", "v199", "v200", "v201", "v202", "v203", "v204", "v205", "v206", "v207", "v208", "v209", "v210", "v211", "v212", "v213", "v214", "v215", "v216", "v217", "v218", "v219", "v220", "v221", "v222", "v223", "v224", "v225", "v226", "v227", "v228", "v229", "v230", "v231", "v232", "v233", "v234", "v235", "v236", "v237", "v238", "v239", "v240", "v241", "v242", "v243", "v244", "v245", "v246", "v247", "v248", "v249", "v250", "v251", "v252", "v253", "v254", "v255", "a0", "a1", "a2", "a3", "a4", "a5", "a6", "a7", "a8", "a9", "a10", "a11", "a12", "a13", "a14", "a15", "a16", "a17", "a18", "a19", "a20", "a21", "a22", "a23", "a24", "a25", "a26", "a27", "a28", "a29", "a30", "a31", "a32", "a33", "a34", "a35", "a36", "a37", "a38", "a39", "a40", "a41", "a42", "a43", "a44", "a45", "a46", "a47", "a48", "a49", "a50", "a51", "a52", "a53", "a54", "a55", "a56", "a57", "a58", "a59", "a60", "a61", "a62", "a63", "a64", "a65", "a66", "a67", "a68", "a69", "a70", "a71", "a72", "a73", "a74", "a75", "a76", "a77", "a78", "a79", "a80", "a81", "a82", "a83", "a84", "a85", "a86", "a87", "a88", "a89", "a90", "a91", "a92", "a93", "a94", "a95", "a96", "a97", "a98", "a99", "a100", "a101", "a102", "a103", "a104", "a105", "a106", "a107", "a108", "a109", "a110", "a111", "a112", "a113", "a114", "a115", "a116", "a117", "a118", "a119", "a120", "a121", "a122", "a123", "a124", "a125", "a126", "a127", "a128", "a129", "a130", "a131", "a132", "a133", "a134", "a135", "a136", "a137", "a138", "a139", "a140", "a141", "a142", "a143", "a144", "a145", "a146", "a147", "a148", "a149", "a150", "a151", "a152", "a153", "a154", "a155", "a156", "a157", "a158", "a159", "a160", "a161", "a162", "a163", "a164", "a165", "a166", "a167", "a168", "a169", "a170", "a171", "a172", "a173", "a174", "a175", "a176", "a177", "a178", "a179", "a180", "a181", "a182", "a183", "a184", "a185", "a186", "a187", "a188", "a189", "a190", "a191", "a192", "a193", "a194", "a195", "a196", "a197", "a198", "a199", "a200", "a201", "a202", "a203", "a204", "a205", "a206", "a207", "a208", "a209", "a210", "a211", "a212", "a213", "a214", "a215", "a216", "a217", "a218", "a219", "a220", "a221", "a222", "a223", "a224", "a225", "a226", "a227", "a228", "a229", "a230", "a231", "a232", "a233", "a234", "a235", "a236", "a237", "a238", "a239", "a240", "a241", "a242", "a243", "a244", "a245", "a246", "a247", "a248", "a249", "a250", "a251", "a252", "a253", "a254", "a255", "s3", "s4", "s5", "s6", "s7", "s8", "s9", "s10", "s11", "s12", "s13", "s14", "s15", "s16", "s17", "s18", "s19", "s20", "s21", "s22", "s23", "s24", "s25", "s26", "s27", "s28", "s29", "s30", "s31", "s33", "s34", "s35", "s36", "s37", "s38", "s39", "s40", "s41", "s42", "s43", "s44", "s45", "s46", "s47", "s48", "s49", "s50", "s51", "s52", "s53", "s54", "s55", "s56", "s57", "s58", "s59", "s60", "s61", "s62", "s63", "s64", "s65", "s66", "s67", "s68", "s69", "s70", "s71", "s72", "s73", "s74", "s75", "s76", "s77", "s78", "s79", "s80", "s81", "s82", "s83", "s84", "s85", "s86", "s87", "s88", "s89", "s90", "s91", "s92", "s93", "s94", "s95", "s96", "s97", "s98", "s99", "vcc", "scc", "memory");
}
extern "C" void kernel_launch(void* const* d_in, const int* in_sizes, int n_in, void* d_out, int out_size, void* d_ws, size_t ws_size, hipStream_t stream) {
  Params p;
  p.x = d_in[0]; p.lar = d_in[1]; p.aim = d_in[2]; p.cr = d_in[3]; p.ci = d_in[4]; p.ldt = d_in[5]; p.dsk = d_in[6];
  p.w1 = d_in[8]; p.w3 = d_in[9]; p.w2 = d_in[10]; p.ws = (char*)d_ws + WS_BASE(ws_size); p.out = d_out;
  (void)hipFuncSetAttribute((const void*)s4d_swiglu_fused, hipFuncAttributeMaxDynamicSharedMemorySize, 163840);
  s4d_swiglu_fused<<<256, 256, 163840, stream>>>(p);
}
```
